```python
import torch

def _softmax(x, axis=-1):
    return torch.softmax(x, dim=axis)
def _gelu(x):
    return torch.nn.functional.gelu(x, approximate="tanh")
def _silu(x):
    return torch.nn.functional.silu(x)
def _sigmoid(x):
    return torch.sigmoid(x)
def _layernorm(x, axis=-1, eps=1e-5):
    m = x.mean(dim=axis, keepdim=True)
    v = x.var(dim=axis, keepdim=True, unbiased=False)
    return (x - m) / torch.sqrt(v + eps)
def _scatter_add(tgt, idx, src, axis=0):
    return tgt.clone().index_add_(axis, idx, src)
def _segment_sum(src, seg, n):
    out = torch.zeros((n,) + src.shape[1:], dtype=src.dtype)
    return out.index_add_(0, seg, src)
def _pad(x, pw):
    flat = tuple(v for pair in reversed(pw) for v in pair)
    return torch.nn.functional.pad(x, flat)

kernel_name = 'recurrent_scan__ssm_conv_h3__glu_gated_reglu__prompt'

def setup_inputs():

    _w = (lambda a: (lambda t: t.to(torch.bfloat16) if t.is_floating_point() else t)(torch.as_tensor(a)))
    def _A_setup():
        B, S, D = 1, 2048, 448
        ks = 4
        return {
            "q": _w((lambda *s: torch.randn(*s, dtype=torch.bfloat16) * 0.05)(B, S, D)),
            "k": _w((lambda *s: torch.randn(*s, dtype=torch.bfloat16) * 0.8)(B, S, D)),
            "v": _w((lambda *s: torch.randn(*s, dtype=torch.bfloat16) * 0.8)(B, S, D)),
            "shift_k": _w((lambda *s: torch.randn(*s, dtype=torch.bfloat16) * 0.8)(D, ks) * 0.5),
            "ssm_k": _w((lambda *s: torch.randn(*s, dtype=torch.bfloat16) * 0.8)(D, S) * (1.0 / S ** 0.5)),
            "ssm_D": _w((lambda *s: torch.randn(*s, dtype=torch.bfloat16) * 0.8)(D)),
        }
    _A_in = _A_setup()

    def _B_setup():
        B, S, D, F = 1, 2048, 448, 1792
        return {
            "x": _w((lambda *s: torch.randn(*s, dtype=torch.bfloat16) * 0.05)(B, S, D)),
            "W_gate": _w((lambda *s: torch.randn(*s, dtype=torch.bfloat16) * 0.2)(D, F) * (2.0 / D) ** 0.5),
            "W_up": _w((lambda *s: torch.randn(*s, dtype=torch.bfloat16) * 0.2)(D, F) * (2.0 / D) ** 0.5),
            "W_down": _w((lambda *s: torch.randn(*s, dtype=torch.bfloat16) * 0.2)(F, D) * (1.0 / F) ** 0.5),
        }
    _B_in = _B_setup()

    return {
        "A_q": _A_in["q"],
        "A_k": _A_in["k"],
        "A_v": _A_in["v"],
        "A_shift_k": _A_in["shift_k"],
        "A_ssm_k": _A_in["ssm_k"],
        "A_ssm_D": _A_in["ssm_D"],
        "B_x": _B_in["x"],
        "B_W_gate": _B_in["W_gate"],
        "B_W_up": _B_in["W_up"],
        "B_W_down": _B_in["W_down"],
    }

def reference(A_q, A_k, A_v, A_shift_k, A_ssm_k, A_ssm_D, B_x, B_W_gate, B_W_up, B_W_down):
    def _A_ref(q, k, v, shift_k, ssm_k, ssm_D):
        B, S, D = q.shape
        ks = shift_k.shape[-1]
        k_pad = _pad(k, ((0, 0), (ks - 1, 0), (0, 0)))
        k_shift = torch.zeros_like(k)
        for t in range(ks):
            k_shift = k_shift + k_pad[:, t:t + S, :] * shift_k[:, ks - 1 - t][None, None, :]
        kv = k_shift * v
        L = 2 * S
        kv_pad = _pad(kv, ((0, 0), (0, S), (0, 0)))
        kf_pad = _pad(ssm_k, ((0, 0), (0, S)))
        kv_f = (lambda x, axes=(-2,-1): torch.fft.rfft2(x.float(), dim=axes))(kv_pad[:, None, :, :], axes=(1, 2))
        kf_f = (lambda x, axes=(-2,-1): torch.fft.rfft2(x.float(), dim=axes))(kf_pad[:, None, :], axes=(1, 2))
        kf_f = (lambda x, *d: x.permute(*d))(kf_f, 1, 2, 0)
        y_f = kv_f * kf_f[None, :, :, :]
        y = (lambda x, s, axes=(-2,-1): torch.fft.irfft2(x, s=s, dim=axes).to(torch.bfloat16))(y_f, (1, L), axes=(1, 2))[:, 0, :S, :]
        y = y + kv * ssm_D[None, None, :]
        return q * y

    def _B_ref(x, W_gate, W_up, W_down):
        gate = torch.einsum("bsd,df->bsf", x, W_gate)
        up = torch.einsum("bsd,df->bsf", x, W_up)
        gate = gate * (lambda x, dt: x.to(dt))(gate > 0, gate.dtype)
        h = gate * up
        return torch.einsum("bsf,fd->bsd", h, W_down)

    x = A_q
    xn = _layernorm(x, axis=-1)
    s = _A_ref(xn, A_k, A_v, A_shift_k, A_ssm_k, A_ssm_D)
    g = _silu(_B_ref(xn, B_W_gate, B_W_up, B_W_down))
    return x + s * g
```

```cpp
#include <hip/hip_runtime.h>
#include <hip/hip_bf16.h>
#include <stdint.h>

typedef unsigned short u16;
static constexpr int S = 2048, D = 448, F = 1792;

__device__ __forceinline__ float bf2f(u16 b) { return __uint_as_float(((unsigned)b) << 16); }
__device__ __forceinline__ u16 f2bf(float f) { __hip_bfloat16 h = __float2bfloat16(f); return *(u16*)&h; }
__device__ __forceinline__ float rbf(float f) { return bf2f(f2bf(f)); }

__global__ void k_ln(const u16* q, u16* xn) {
    int s = blockIdx.x; int t = threadIdx.x;
    __shared__ float red[256];
    float sum = 0;
    for (int d = t; d < D; d += 256) sum += bf2f(q[s * D + d]);
    red[t] = sum; __syncthreads();
    for (int o = 128; o > 0; o >>= 1) { if (t < o) red[t] += red[t + o]; __syncthreads(); }
    float m = red[0] / D; __syncthreads();
    float sq = 0;
    for (int d = t; d < D; d += 256) { float x = bf2f(q[s * D + d]) - m; sq += x * x; }
    red[t] = sq; __syncthreads();
    for (int o = 128; o > 0; o >>= 1) { if (t < o) red[t] += red[t + o]; __syncthreads(); }
    float var = red[0] / D;
    float mb = rbf(m), vb = rbf(var);
    float den = rbf(sqrtf(rbf(vb + 1e-5f)));
    for (int d = t; d < D; d += 256) {
        float dd = rbf(bf2f(q[s * D + d]) - mb);
        xn[s * D + d] = f2bf(dd / den);
    }
}
__global__ void k_kv(const u16* k, const u16* v, const u16* sk, u16* kv) {
    int i = blockIdx.x * 256 + threadIdx.x; if (i >= S * D) return;
    int s = i / D, d = i % D;
    float acc = 0;
    for (int t = 0; t < 4; ++t) {
        int ss = s + t - 3;
        float kk = ss >= 0 ? bf2f(k[ss * D + d]) : 0.f;
        float p = rbf(kk * bf2f(sk[d * 4 + 3 - t]));
        acc = rbf(acc + p);
    }
    kv[i] = f2bf(acc * bf2f(v[i]));
}
__global__ void k_conv(const u16* kv, const u16* h, const u16* Dv, u16* y) {
    int i = blockIdx.x * 256 + threadIdx.x; if (i >= S * D) return;
    int s = i / D, d = i % D;
    float acc = 0;
    for (int j = 0; j <= s; ++j) acc += bf2f(kv[(s - j) * D + d]) * bf2f(h[d * S + j]);
    float yy = rbf(acc);
    float t = rbf(bf2f(kv[i]) * bf2f(Dv[d]));
    y[i] = f2bf(yy + t);
}
__global__ void k_g1(const u16* xn, const u16* wg, const u16* wu, u16* hh) {
    int i = blockIdx.x * 256 + threadIdx.x; if (i >= S * F) return;
    int s = i / F, f = i % F;
    float g = 0, u = 0;
    for (int k = 0; k < D; ++k) { float x = bf2f(xn[s * D + k]); g += x * bf2f(wg[k * F + f]); u += x * bf2f(wu[k * F + f]); }
    g = rbf(g); u = rbf(u);
    g = g > 0 ? g : 0.f;
    hh[i] = f2bf(g * u);
}
__global__ void k_g2(const u16* hh, const u16* wd, const u16* q, const u16* xn, const u16* y, u16* out) {
    int i = blockIdx.x * 256 + threadIdx.x; if (i >= S * D) return;
    int s = i / D, d = i % D;
    float o = 0;
    for (int f = 0; f < F; ++f) o += bf2f(hh[s * F + f]) * bf2f(wd[f * D + d]);
    o = rbf(o);
    float g = rbf(o / (1.f + __expf(-o)));
    float sv = rbf(bf2f(xn[i]) * bf2f(y[i]));
    out[i] = f2bf(bf2f(q[i]) + rbf(sv * g));
}

extern "C" void kernel_launch(void* const* d_in, const int* in_sizes, int n_in,
                              void* d_out, int out_size, void* d_ws, size_t ws_size,
                              hipStream_t stream) {
    const u16* q = (const u16*)d_in[0];
    const u16* k = (const u16*)d_in[1];
    const u16* v = (const u16*)d_in[2];
    const u16* sk = (const u16*)d_in[3];
    const u16* ssm = (const u16*)d_in[4];
    const u16* Dv = (const u16*)d_in[5];
    const u16* wg = (const u16*)d_in[7];
    const u16* wu = (const u16*)d_in[8];
    const u16* wd = (const u16*)d_in[9];
    u16* ws = (u16*)d_ws;
    u16* xn = ws; u16* kv = xn + S * D; u16* y = kv + S * D; u16* hh = y + S * D;
    k_ln<<<S, 256, 0, stream>>>(q, xn);
    k_kv<<<S * D / 256, 256, 0, stream>>>(k, v, sk, kv);
    k_conv<<<S * D / 256, 256, 0, stream>>>(kv, ssm, Dv, y);
    k_g1<<<S * F / 256, 256, 0, stream>>>(xn, wg, wu, hh);
    k_g2<<<S * D / 256, 256, 0, stream>>>(hh, wd, q, xn, y, (u16*)d_out);
}
```

```cpp
#include <hip/hip_runtime.h>
#include <stdint.h>

typedef unsigned short u16;
typedef __attribute__((ext_vector_type(4))) short s16x4;
typedef __attribute__((ext_vector_type(8))) short s16x8;
typedef __attribute__((ext_vector_type(8))) __bf16 bf16x8;
typedef __attribute__((ext_vector_type(2))) __bf16 bf16x2;
typedef __attribute__((ext_vector_type(16))) float f32x16;
typedef __attribute__((ext_vector_type(4))) float f32x4;
typedef __attribute__((ext_vector_type(4))) unsigned u32x4;
typedef __attribute__((ext_vector_type(2))) unsigned u32x2;
#define GAS __attribute__((address_space(1)))
#define LAS __attribute__((address_space(3)))
#define DI __device__ __forceinline__

static constexpr int S = 2048, D = 448, F = 1792;

static constexpr size_t WS_FLAGS = 0;
static constexpr size_t WS_STATS = 4096;
static constexpr size_t WS_HT    = WS_STATS + (size_t)S * 8;
static constexpr size_t WS_WDT   = WS_HT + (size_t)S * F * 2;
static constexpr size_t WS_KVR   = WS_WDT + (size_t)F * D * 2;
static constexpr size_t WS_YT    = WS_KVR + (size_t)S * D * 2;
static constexpr size_t WS_END   = WS_YT + (size_t)S * D * 2;

DI float bflo(unsigned u) { return __uint_as_float(u << 16); }
DI float bfhi(unsigned u) { return __uint_as_float(u & 0xffff0000u); }
DI unsigned pk2(float a, float b) { bf16x2 r = {(__bf16)a, (__bf16)b}; return __builtin_bit_cast(unsigned, r); }
DI float rbf(float a) { return bflo(pk2(a, a)); }
DI float dot2ones(unsigned u, float c) {
    bf16x2 ones = {(__bf16)1.0f, (__bf16)1.0f};
    return __builtin_amdgcn_fdot2_f32_bf16(__builtin_bit_cast(bf16x2, u), ones, c, false);
}
DI f32x16 mfma32(s16x8 a, s16x8 b, f32x16 c) {
    return __builtin_amdgcn_mfma_f32_32x32x16_bf16(__builtin_bit_cast(bf16x8, a), __builtin_bit_cast(bf16x8, b), c, 0, 0, 0);
}
DI f32x4 mfma16(s16x8 a, s16x8 b, f32x4 c) {
    return __builtin_amdgcn_mfma_f32_16x16x32_bf16(__builtin_bit_cast(bf16x8, a), __builtin_bit_cast(bf16x8, b), c, 0, 0, 0);
}
#define WAIT_V(n) asm volatile("s_waitcnt vmcnt(" #n ")" ::: "memory")
#define BAR() do { asm volatile("s_waitcnt lgkmcnt(0)" ::: "memory"); __builtin_amdgcn_s_barrier(); } while (0)

struct K1Args {
    const u16* q; const u16* k; const u16* v; const u16* sk;
    const u16* wg; const u16* wu; const u16* wd;
    char* ws;
};

static constexpr int K1_STAGE = 49152;
static constexpr int K1_LDS = 3 * K1_STAGE;

DI void dma16(const GAS char* src, LAS char* dst) {
    __builtin_amdgcn_global_load_lds((const GAS void*)src, (LAS void*)dst, 16, 0, 0);
}

__device__ void prep_block(const K1Args& a, int pb) {
    const int tid = threadIdx.x, lane = tid & 63, w = tid >> 6;
    if (pb == 0 && tid < 8) ((unsigned*)(a.ws + WS_FLAGS))[tid * 64] = 0u;
    const int srow = pb * 64 + w * 8 + (lane >> 3);
    u32x4 xs[7];
#pragma unroll
    for (int i = 0; i < 7; ++i)
        xs[i] = *(const u32x4*)(a.q + (size_t)srow * D + ((lane & 7) + 8 * i) * 8);
    const int s0 = pb * 64 + (lane & 7) * 8;
    const int dch = (8 * w + (lane >> 3)) * 8;
    u32x4 kr[11], vr[8], skr[4];
    if (w < 7) {
#pragma unroll
        for (int i = 0; i < 11; ++i) {
            int s = s0 - 3 + i;
            kr[i] = (s >= 0) ? *(const u32x4*)(a.k + (size_t)s * D + dch) : u32x4{0, 0, 0, 0};
        }
#pragma unroll
        for (int i = 0; i < 8; ++i) vr[i] = *(const u32x4*)(a.v + (size_t)(s0 + i) * D + dch);
#pragma unroll
        for (int i = 0; i < 4; ++i) skr[i] = *(const u32x4*)(a.sk + (size_t)dch * 4 + i * 8);
    }
    u32x4 wr[8];
    const int item = pb * 392 + tid;
    const int oct = item % 56, hk = item / 56, half = hk & 1, ks = hk >> 1;
    if (tid < 392) {
#pragma unroll
        for (int j = 0; j < 8; ++j) {
            int f = 16 * ks + 8 * (j >> 2) + 4 * half + (j & 3);
            wr[j] = *(const u32x4*)(a.wd + (size_t)f * D + oct * 8);
        }
    }
    {
        float sum = 0.f;
#pragma unroll
        for (int i = 0; i < 7; ++i)
#pragma unroll
            for (int j = 0; j < 4; ++j) sum += bflo(xs[i][j]) + bfhi(xs[i][j]);
        sum += __shfl_xor(sum, 1); sum += __shfl_xor(sum, 2); sum += __shfl_xor(sum, 4);
        const float m = sum * (1.0f / D);
        float sq = 0.f;
#pragma unroll
        for (int i = 0; i < 7; ++i)
#pragma unroll
            for (int j = 0; j < 4; ++j) {
                float d0 = bflo(xs[i][j]) - m, d1 = bfhi(xs[i][j]) - m;
                sq += d0 * d0; sq += d1 * d1;
            }
        sq += __shfl_xor(sq, 1); sq += __shfl_xor(sq, 2); sq += __shfl_xor(sq, 4);
        const float var = sq * (1.0f / D);
        const float mb = rbf(m), vb = rbf(var);
        const float den = rbf(sqrtf(rbf(vb + 1e-5f)));
        if ((lane & 7) == 0) ((float2*)(a.ws + WS_STATS))[srow] = make_float2(mb, 1.0f / den);
    }
    if (w < 7) {
        u16* kvr = (u16*)(a.ws + WS_KVR);
#pragma unroll
        for (int c = 0; c < 8; ++c) {
            const unsigned t01 = skr[c >> 1][(c & 1) * 2], t23 = skr[c >> 1][(c & 1) * 2 + 1];
            const float w0 = bflo(t01), w1 = bfhi(t01), w2 = bflo(t23), w3 = bfhi(t23);
            float kf[11];
#pragma unroll
            for (int i = 0; i < 11; ++i) kf[i] = (c & 1) ? bfhi(kr[i][c >> 1]) : bflo(kr[i][c >> 1]);
            float o[8];
#pragma unroll
            for (int i = 0; i < 8; ++i) {
                float acc = rbf(kf[i] * w3);
                acc = rbf(acc + rbf(kf[i + 1] * w2));
                acc = rbf(acc + rbf(kf[i + 2] * w1));
                acc = rbf(acc + rbf(kf[i + 3] * w0));
                float vv = (c & 1) ? bfhi(vr[i][c >> 1]) : bflo(vr[i][c >> 1]);
                o[i] = acc * vv;
            }
            u32x4 pk = {pk2(o[7], o[6]), pk2(o[5], o[4]), pk2(o[3], o[2]), pk2(o[1], o[0])};
            *(u32x4*)(kvr + (size_t)(dch + c) * S + (2040 - s0)) = pk;
        }
    }
    if (tid < 392) {
        u16* wdt = (u16*)(a.ws + WS_WDT);
        const int dt = oct >> 2;
#pragma unroll
        for (int e = 0; e < 8; ++e) {
            u32x4 o;
#pragma unroll
            for (int jj = 0; jj < 4; ++jj) {
                unsigned lo = wr[2 * jj][e >> 1], hi = wr[2 * jj + 1][e >> 1];
                o[jj] = (e & 1) ? __builtin_amdgcn_perm(hi, lo, 0x07060302u) : __builtin_amdgcn_perm(hi, lo, 0x05040100u);
            }
            const int dl = (oct & 3) * 8 + e;
            *(u32x4*)(wdt + (((size_t)dt * 112 + ks) * 64 + half * 32 + dl) * 8) = o;
        }
    }
}

__global__ __launch_bounds__(512, 2) void k1_kernel(K1Args a) {
    extern __shared__ __attribute__((aligned(16))) char smem_[];
    LAS char* smem = (LAS char*)smem_;
    const int bid = blockIdx.x;
    if (bid >= 224) { prep_block(a, bid - 224); return; }
    const int tid = threadIdx.x, lane = tid & 63;
    const int w = __builtin_amdgcn_readfirstlane(tid >> 6);
    const int xcd = bid & 7, idx = bid >> 3;
    const int rb = (xcd >> 1) * 4 + (idx & 3);
    const int cb = (xcd & 1) * 7 + (idx >> 2);
    const int wm = w >> 2, wn = w & 3;

    const GAS char* qb = (const GAS char*)a.q + (size_t)rb * 128 * D * 2;
    const GAS char* wgb = (const GAS char*)a.wg + (size_t)cb * 256;
    const GAS char* wub = (const GAS char*)a.wu + (size_t)cb * 256;
    unsigned xoff[2];
#pragma unroll
    for (int e = 0; e < 2; ++e) {
        const int i = 2 * w + e;
        const int row = 8 * i + (lane >> 3);
        const int p = (lane & 7) ^ ((row >> 1) & 7);
        xoff[e] = row * (D * 2) + p * 16;
    }
    unsigned woff[2];
#pragma unroll
    for (int gu = 0; gu < 2; ++gu) {
        const int piece = (lane & 15) ^ (((lane >> 4) << 2) | (gu << 1));
        woff[gu] = (lane >> 4) * (F * 2) + piece * 16 + (unsigned)(8 * w) * (F * 2);
    }
    auto stage = [&](int c, int slot) {
        LAS char* sb = smem + slot * K1_STAGE;
#pragma unroll
        for (int e = 0; e < 2; ++e) dma16(qb + c * 128 + xoff[e], sb + (2 * w + e) * 1024);
#pragma unroll
        for (int gu = 0; gu < 2; ++gu)
#pragma unroll
            for (int e = 0; e < 2; ++e)
                dma16((gu ? wub : wgb) + (size_t)(c * 64 + 4 * e) * (F * 2) + woff[gu], sb + 16384 + gu * 16384 + (2 * w + e) * 1024);
    };
    stage(0, 0); stage(1, 1); stage(2, 2);

    const int r = lane & 31, half = lane >> 5;
    unsigned xa[4];
    unsigned wa[2][4];
    const int gi = lane >> 4, blk = gi & 1, q4 = (lane >> 2) & 3, p4 = lane & 3, gu = p4 >> 1;
    const int ct0 = 2 * wn + wm, ct1 = 2 * wn + 1 - wm;
#pragma unroll
    for (int t = 0; t < 4; ++t) {
        const int tr = (t + wn) & 3;
        xa[t] = (unsigned)(wm * 64 + r) * 128 + (((2 * tr + half) ^ ((r >> 1) & 7)) * 16);
#pragma unroll
        for (int i = 0; i < 2; ++i) {
            const int ct = i ? ct1 : ct0;
            const int piece = 2 * ct + blk;
            wa[i][t] = 16384 + gu * 16384 + (16 * tr + 8 * half + q4) * 256 + ((piece ^ ((q4 << 2) | (gu << 1))) * 16) + (p4 & 1) * 8;
        }
    }
    f32x16 acc[2][2];
#pragma unroll
    for (int i = 0; i < 2; ++i)
#pragma unroll
        for (int j = 0; j < 2; ++j)
#pragma unroll
            for (int e = 0; e < 16; ++e) acc[i][j][e] = 0.f;
    float xsum[2] = {0.f, 0.f}, csum = 0.f;

#pragma unroll
    for (int c = 0; c < 7; ++c) {
        if (c == 0) WAIT_V(12); else if (c == 6) WAIT_V(0); else WAIT_V(6);
        __builtin_amdgcn_s_barrier();
        if (c >= 1 && c + 2 < 7) stage(c + 2, (c + 2) % 3);
        LAS char* sb = smem + (c % 3) * K1_STAGE;
#pragma unroll
        for (int t = 0; t < 4; ++t) {
            s16x8 xf[2], wf[2];
#pragma unroll
            for (int j = 0; j < 2; ++j) xf[j] = *(const LAS s16x8*)(sb + xa[t] + j * 4096);
#pragma unroll
            for (int i = 0; i < 2; ++i) {
                s16x4 lo = __builtin_amdgcn_ds_read_tr16_b64_v4i16((LAS s16x4*)(sb + wa[i][t]));
                s16x4 hi = __builtin_amdgcn_ds_read_tr16_b64_v4i16((LAS s16x4*)(sb + wa[i][t] + 1024));
                wf[i] = __builtin_shufflevector(lo, hi, 0, 1, 2, 3, 4, 5, 6, 7);
            }
#pragma unroll
            for (int i = 0; i < 2; ++i)
#pragma unroll
                for (int j = 0; j < 2; ++j) acc[i][j] = mfma32(wf[i], xf[j], acc[i][j]);
            {
                u32x4 u = __builtin_bit_cast(u32x4, wf[0]);
#pragma unroll
                for (int e = 0; e < 4; ++e) csum = dot2ones(u[e], csum);
            }
            if (t == 0) {
#pragma unroll
                for (int j = 0; j < 2; ++j) {
                    u32x4 u = __builtin_bit_cast(u32x4, xf[j]);
#pragma unroll
                    for (int e = 0; e < 4; ++e) xsum[j] = dot2ones(u[e], xsum[j]);
                }
            }
        }
    }
    LAS float* xs = (LAS float*)(smem + K1_STAGE);
    LAS float* cs = (LAS float*)(smem + K1_STAGE + 4096);
#pragma unroll
    for (int j = 0; j < 2; ++j) xs[((2 * wm + j) * 32 + r) * 8 + wn * 2 + half] = xsum[j];
    csum += __shfl_xor(csum, 32);
    if (half == 0) cs[ct0 * 32 + r] = csum;
    BAR();
    float mean[2];
#pragma unroll
    for (int j = 0; j < 2; ++j) {
        const LAS f32x4* p = (const LAS f32x4*)(xs + ((2 * wm + j) * 32 + r) * 8);
        f32x4 s0 = p[0], s1 = p[1];
        float s = ((s0[0] + s0[1]) + (s0[2] + s0[3])) + ((s1[0] + s1[1]) + (s1[2] + s1[3]));
        mean[j] = rbf(s * (1.0f / D));
    }
    u16* hT = (u16*)(a.ws + WS_HT);
#pragma unroll
    for (int i = 0; i < 2; ++i) {
        const int ct = i ? ct1 : ct0;
        f32x4 cv[4];
#pragma unroll
        for (int g = 0; g < 4; ++g) cv[g] = *(const LAS f32x4*)(cs + ct * 32 + 8 * g + 4 * half);
#pragma unroll
        for (int j = 0; j < 2; ++j) {
            float hv[8];
#pragma unroll
            for (int e = 0; e < 4; ++e) {
                float g0 = acc[i][j][e] - mean[j] * cv[0][e];
                float u0 = acc[i][j][4 + e] - mean[j] * cv[1][e];
                float g1 = acc[i][j][8 + e] - mean[j] * cv[2][e];
                float u1 = acc[i][j][12 + e] - mean[j] * cv[3][e];
                hv[e] = fmaxf(g0, 0.f) * u0;
                hv[4 + e] = fmaxf(g1, 0.f) * u1;
            }
            u32x4 o = {pk2(hv[0], hv[1]), pk2(hv[2], hv[3]), pk2(hv[4], hv[5]), pk2(hv[6], hv[7])};
            const int T = rb * 4 + 2 * wm + j, KS = cb * 8 + ct;
            *(u32x4*)(hT + (((size_t)T * 112 + KS) * 64 + lane) * 8) = o;
        }
    }
}

struct K2Args {
    const u16* q; const u16* ssm; const u16* Dv;
    char* ws; u16* out;
};
static constexpr int HC_STRIDE = 4160;
static constexpr int CH_BYTES = 8 * HC_STRIDE + 4608;
static constexpr int K2_CRED = 2 * CH_BYTES;
static constexpr int K2_YBUF = K2_CRED + 65536;
static constexpr int K2_FLAG = K2_YBUF + 8192;
static constexpr int K2_LDS = K2_FLAG + 16;
static constexpr int RED_STRIDE = 272;
static constexpr int WIN = 8;

__global__ __launch_bounds__(512, 2) void k2_kernel(K2Args a) {
    extern __shared__ __attribute__((aligned(16))) char smem_[];
    LAS char* smem = (LAS char*)smem_;
    const int bid = blockIdx.x;
    const int tid = threadIdx.x, lane = tid & 63;
    const int w = __builtin_amdgcn_readfirstlane(tid >> 6);
    const int xcd = bid & 7, idx = bid >> 3;
    const int rt = xcd * 4 + (idx & 3);
    const int ctile = idx >> 2;
    const int chl = tid >> 8, u = tid & 255;
    const int ch = 2 * bid + chl;

    const u16* hsrc = a.ssm + (size_t)ch * S;
    const u32x4 h0 = *(const u32x4*)(hsrc + u * 8);
    const u32x4 h1 = (u < 255) ? *(const u32x4*)(hsrc + u * 8 + 8) : u32x4{0, 0, 0, 0};
    const u32x4 xr = *(const u32x4*)((const u16*)(a.ws + WS_KVR) + (size_t)ch * S + u * 8);
    const float Dval = bflo((unsigned)a.Dv[2 * bid + (w >> 2)]);

    const u32x4* hp = (const u32x4*)(a.ws + WS_HT) + ((size_t)(rt * 2) * 112 + 14 * w) * 64 + lane;
    const u32x4* wp = (const u32x4*)(a.ws + WS_WDT) + ((size_t)(ctile * 2) * 112 + 14 * w) * 64 + lane;
    u32x4 win[WIN][4];
#pragma unroll
    for (int i = 0; i < WIN; ++i) {
        win[i][0] = wp[i * 64]; win[i][1] = wp[(112 + i) * 64];
        win[i][2] = hp[i * 64]; win[i][3] = hp[(112 + i) * 64];
    }
    __builtin_amdgcn_sched_barrier(0);

    {
        LAS char* cb = smem + chl * CH_BYTES;
        unsigned d[8] = {h0[0], h0[1], h0[2], h0[3], h1[0], h1[1], h1[2], h1[3]};
        unsigned al[7];
#pragma unroll
        for (int i = 0; i < 7; ++i) al[i] = __builtin_amdgcn_alignbit(d[i + 1], d[i], 16);
        const int t = u + 2;
#pragma unroll
        for (int sg = 0; sg < 8; ++sg) {
            u32x4 o;
#pragma unroll
            for (int e = 0; e < 4; ++e) o[e] = (sg & 1) ? al[(sg >> 1) + e] : d[(sg >> 1) + e];
            *(LAS u32x4*)(cb + sg * HC_STRIDE + t * 16) = o;
        }
        if (u < 16) {
            const int sg = u >> 1, sl = u & 1;
            u32x4 o = {0, 0, 0, 0};
            *(LAS u32x4*)(cb + sg * HC_STRIDE + sl * 16) = o;
        }
        *(LAS u32x4*)(cb + 8 * HC_STRIDE + u * 16) = xr;
        if (u < 32) *(LAS u32x4*)(cb + 8 * HC_STRIDE + 4096 + u * 16) = u32x4{0, 0, 0, 0};
    }
    BAR();
    if (u == 0) {
        LAS u16* cb = (LAS u16*)(smem + chl * CH_BYTES);
#pragma unroll
        for (int sg = 1; sg < 8; ++sg)
#pragma unroll
            for (int j = 8 - sg; j < 8; ++j) {
                const int hi = sg + j - 8;
                const unsigned v = h0[hi >> 1];
                cb[(sg * HC_STRIDE + 16) / 2 + j] = (u16)((hi & 1) ? (v >> 16) : (v & 0xffff));
            }
    }
    BAR();
    {
        const int cw = w >> 2, qp = w & 3;
        LAS char* cb = smem + cw * CH_BYTES;
        const int m = lane & 15, g = lane >> 4;
        const int sg = m & 7, be = (m == 0) ? 2 : (m >> 3);
        f32x4 cacc[8];
#pragma unroll
        for (int c = 0; c < 8; ++c) cacc[c] = f32x4{0.f, 0.f, 0.f, 0.f};
#pragma unroll
        for (int qq = 0; qq < 2; ++qq) {
            const int q = 2 * qp + qq;
            LAS char* ab = cb + sg * HC_STRIDE + (4 * q + g + be) * 16;
            LAS char* gb = cb + 8 * HC_STRIDE + (2032 - 256 * 7 + 32 * q - 16 * m + 8 * g) * 2;
            s16x8 A[8], G[8];
#pragma unroll
            for (int p = 0; p < 8; ++p) A[p] = *(const LAS s16x8*)(ab + p * 512);
#pragma unroll
            for (int mm = 0; mm < 8; ++mm) G[mm] = *(const LAS s16x8*)(gb + (7 - mm) * 512);
#pragma unroll
            for (int p = 0; p < 8; ++p)
#pragma unroll
                for (int c = p; c < 8; ++c) cacc[c] = mfma16(A[p], G[c - p], cacc[c]);
        }
        LAS f32x4* red = (LAS f32x4*)(smem + K2_CRED) + w * 512;
#pragma unroll
        for (int c = 0; c < 8; ++c) red[c * 64 + lane] = cacc[c];
    }
    BAR();
    {
        const int cw = w >> 2, j = w & 3;
        const int m0 = lane >> 4, ap = lane & 15;
        const LAS f32x4* red = (const LAS f32x4*)(smem + K2_CRED) + cw * 4 * 512;
        const LAS u16* xrl = (const LAS u16*)(smem + cw * CH_BYTES + 8 * HC_STRIDE);
        LAS u16* yb = (LAS u16*)(smem + K2_YBUF) + cw * 2048;
#pragma unroll
        for (int cc = 0; cc < 2; ++cc) {
            const int c = 2 * j + cc;
            f32x4 s = red[c * 64 + lane];
#pragma unroll
            for (int ww = 1; ww < 4; ++ww) s += red[ww * 512 + c * 64 + lane];
#pragma unroll
            for (int e = 0; e < 4; ++e) {
                const int m = 4 * m0 + e;
                const int b = (m == 0) ? 15 : m - 1;
                const int sidx = 256 * c + 16 * ap + b;
                const float kv = bflo((unsigned)xrl[2047 - sidx]);
                const float y = rbf(s[e]);
                const float t = rbf(kv * Dval);
                yb[sidx] = (u16)(pk2(y + t, 0.f) & 0xffff);
            }
        }
    }
    BAR();
    {
        const u32x4 yv = *(const LAS u32x4*)(smem + K2_YBUF + tid * 16);
        __amdgpu_buffer_rsrc_t rs = __builtin_amdgcn_make_buffer_rsrc((void*)(a.ws + WS_YT), 0, S * D * 2, 0x00020000);
        const unsigned off = (unsigned)(2 * bid) * 4096 + tid * 16;
        __builtin_amdgcn_raw_buffer_store_b128(yv, rs, off, 0, 16);
        WAIT_V(0);
        __builtin_amdgcn_s_barrier();
        if (tid == 0) {
            unsigned* cnt = (unsigned*)(a.ws + WS_FLAGS) + ((2 * bid) >> 6) * 64;
            __hip_atomic_fetch_add(cnt, 1u, __ATOMIC_RELAXED, __HIP_MEMORY_SCOPE_AGENT);
        }
    }
    f32x16 acc[2][2];
#pragma unroll
    for (int i = 0; i < 2; ++i)
#pragma unroll
        for (int j = 0; j < 2; ++j)
#pragma unroll
            for (int e = 0; e < 16; ++e) acc[i][j][e] = 0.f;
#pragma unroll
    for (int i = 0; i < 14; ++i) {
        s16x8 wf0 = __builtin_bit_cast(s16x8, win[i % WIN][0]), wf1 = __builtin_bit_cast(s16x8, win[i % WIN][1]);
        s16x8 hf0 = __builtin_bit_cast(s16x8, win[i % WIN][2]), hf1 = __builtin_bit_cast(s16x8, win[i % WIN][3]);
        if (i + WIN < 14) {
            win[i % WIN][0] = wp[(i + WIN) * 64]; win[i % WIN][1] = wp[(112 + i + WIN) * 64];
            win[i % WIN][2] = hp[(i + WIN) * 64]; win[i % WIN][3] = hp[(112 + i + WIN) * 64];
        }
        acc[0][0] = mfma32(wf0, hf0, acc[0][0]);
        acc[0][1] = mfma32(wf0, hf1, acc[0][1]);
        acc[1][0] = mfma32(wf1, hf0, acc[1][0]);
        acc[1][1] = mfma32(wf1, hf1, acc[1][1]);
        __builtin_amdgcn_sched_barrier(0);
    }
    const int tp = tid >> 4, cq = tid & 15;
    const int tok0 = rt * 64 + 2 * tp, d0 = ctile * 64 + 4 * cq;
    const u32x2 q0 = *(const u32x2*)(a.q + (size_t)tok0 * D + d0);
    const u32x2 q1 = *(const u32x2*)(a.q + (size_t)(tok0 + 1) * D + d0);
    const f32x4 st = *(const f32x4*)(a.ws + WS_STATS + (size_t)tok0 * 8);
    BAR();
    {
        const int r = lane & 31, half = lane >> 5;
        LAS char* rb = smem + w * (64 * RED_STRIDE);
#pragma unroll
        for (int i = 0; i < 2; ++i)
#pragma unroll
            for (int j = 0; j < 2; ++j)
#pragma unroll
                for (int g = 0; g < 4; ++g) {
                    f32x4 v = {acc[i][j][4 * g], acc[i][j][4 * g + 1], acc[i][j][4 * g + 2], acc[i][j][4 * g + 3]};
                    *(LAS f32x4*)(rb + (j * 32 + r) * RED_STRIDE + (i * 32 + 8 * g + 4 * half) * 4) = v;
                }
    }
    if (tid == 0) {
        unsigned* cnt = (unsigned*)(a.ws + WS_FLAGS) + ctile * 64;
        unsigned spins = 0;
        while (__hip_atomic_load(cnt, __ATOMIC_RELAXED, __HIP_MEMORY_SCOPE_AGENT) < 32u) {
            __builtin_amdgcn_s_sleep(1);
            if (++spins > (1u << 20)) break;
        }
    }
    BAR();
    {
        __amdgpu_buffer_rsrc_t rs = __builtin_amdgcn_make_buffer_rsrc((void*)(a.ws + WS_YT), 0, S * D * 2, 0x00020000);
        unsigned yv[4];
#pragma unroll
        for (int j = 0; j < 4; ++j)
            yv[j] = __builtin_amdgcn_raw_buffer_load_b32(rs, (unsigned)(d0 + j) * 4096 + tok0 * 2, 0, 16);
        f32x4 o[2];
#pragma unroll
        for (int e = 0; e < 2; ++e) {
            const LAS char* rb = smem + (2 * tp + e) * RED_STRIDE + cq * 16;
            f32x4 s = *(const LAS f32x4*)rb;
#pragma unroll
            for (int ww = 1; ww < 8; ++ww) s += *(const LAS f32x4*)(rb + ww * (64 * RED_STRIDE));
            o[e] = s;
        }
#pragma unroll
        for (int e = 0; e < 2; ++e) {
            const float mb = st[2 * e], rd = st[2 * e + 1];
            const u32x2 qq = e ? q1 : q0;
            float res[4];
#pragma unroll
            for (int j = 0; j < 4; ++j) {
                const float x = (j & 1) ? bfhi(qq[j >> 1]) : bflo(qq[j >> 1]);
                const float xn = rbf(rbf(x - mb) * rd);
                const float ov = rbf(o[e][j] * rd * rd);
                const float gg = rbf(ov / (1.0f + __expf(-ov)));
                const float y = e ? bfhi(yv[j]) : bflo(yv[j]);
                const float sv = rbf(xn * y);
                res[j] = x + rbf(sv * gg);
            }
            u32x2 ov2 = {pk2(res[0], res[1]), pk2(res[2], res[3])};
            *(u32x2*)(a.out + (size_t)(tok0 + e) * D + d0) = ov2;
        }
    }
}

extern "C" void kernel_launch(void* const* d_in, const int* in_sizes, int n_in,
                              void* d_out, int out_size, void* d_ws, size_t ws_size,
                              hipStream_t stream) {
    static bool attr_set = false;
    if (!attr_set) {
        hipFuncSetAttribute((const void*)k1_kernel, hipFuncAttributeMaxDynamicSharedMemorySize, K1_LDS);
        hipFuncSetAttribute((const void*)k2_kernel, hipFuncAttributeMaxDynamicSharedMemorySize, K2_LDS);
        attr_set = true;
    }
    K1Args a1;
    a1.q = (const u16*)d_in[0]; a1.k = (const u16*)d_in[1]; a1.v = (const u16*)d_in[2]; a1.sk = (const u16*)d_in[3];
    a1.wg = (const u16*)d_in[7]; a1.wu = (const u16*)d_in[8]; a1.wd = (const u16*)d_in[9];
    a1.ws = (char*)d_ws;
    K2Args a2;
    a2.q = (const u16*)d_in[0]; a2.ssm = (const u16*)d_in[4]; a2.Dv = (const u16*)d_in[5];
    a2.ws = (char*)d_ws; a2.out = (u16*)d_out;
    k1_kernel<<<256, 512, K1_LDS, stream>>>(a1);
    k2_kernel<<<224, 512, K2_LDS, stream>>>(a2);
}
```

```cpp
#include <hip/hip_runtime.h>
#include <stdint.h>

typedef unsigned short u16;
typedef __attribute__((ext_vector_type(4))) short s16x4;
typedef __attribute__((ext_vector_type(8))) short s16x8;
typedef __attribute__((ext_vector_type(8))) __bf16 bf16x8;
typedef __attribute__((ext_vector_type(2))) __bf16 bf16x2;
typedef __attribute__((ext_vector_type(16))) float f32x16;
typedef __attribute__((ext_vector_type(4))) float f32x4;
typedef __attribute__((ext_vector_type(4))) unsigned u32x4;
typedef __attribute__((ext_vector_type(2))) unsigned u32x2;
#define GAS __attribute__((address_space(1)))
#define LAS __attribute__((address_space(3)))
#define DI __device__ __forceinline__

static constexpr int S = 2048, D = 448, F = 1792;
#define STAMP_DECL
#define STAMP(kid, i)
#define STAMP_FLUSH(kid)
#define ST_AUX 0
#define PREFETCH 0

static constexpr size_t WS_FLAGS = 0;
static constexpr size_t WS_STATS = 4096;
static constexpr size_t WS_HT    = WS_STATS + (size_t)S * 8;
static constexpr size_t WS_WDT   = WS_HT + (size_t)S * F * 2;
static constexpr size_t WS_KVR   = WS_WDT + (size_t)F * D * 2;
static constexpr size_t WS_YT    = WS_KVR + (size_t)S * D * 2;
static constexpr size_t WS_END   = WS_YT + (size_t)S * D * 2;

DI float bflo(unsigned u) { return __uint_as_float(u << 16); }
DI float bfhi(unsigned u) { return __uint_as_float(u & 0xffff0000u); }
DI unsigned pk2(float a, float b) { bf16x2 r = {(__bf16)a, (__bf16)b}; return __builtin_bit_cast(unsigned, r); }
DI float rbf(float a) { return bflo(pk2(a, a)); }
DI float dot2ones(unsigned u, float c) {
    bf16x2 ones = {(__bf16)1.0f, (__bf16)1.0f};
    return __builtin_amdgcn_fdot2_f32_bf16(__builtin_bit_cast(bf16x2, u), ones, c, false);
}
DI f32x16 mfma32(s16x8 a, s16x8 b, f32x16 c) {
    return __builtin_amdgcn_mfma_f32_32x32x16_bf16(__builtin_bit_cast(bf16x8, a), __builtin_bit_cast(bf16x8, b), c, 0, 0, 0);
}
DI f32x4 mfma16(s16x8 a, s16x8 b, f32x4 c) {
    return __builtin_amdgcn_mfma_f32_16x16x32_bf16(__builtin_bit_cast(bf16x8, a), __builtin_bit_cast(bf16x8, b), c, 0, 0, 0);
}
DI f32x4 mfma16u(u32x4 a, u32x4 b, f32x4 c) { return mfma16(__builtin_bit_cast(s16x8, a), __builtin_bit_cast(s16x8, b), c); }
DI f32x16 mfma32u(u32x4 a, u32x4 b, f32x16 c) { return mfma32(__builtin_bit_cast(s16x8, a), __builtin_bit_cast(s16x8, b), c); }
#define WAIT_V(n) asm volatile("s_waitcnt vmcnt(" #n ")" ::: "memory")
#define BAR() do { asm volatile("s_waitcnt lgkmcnt(0)" ::: "memory"); __builtin_amdgcn_s_barrier(); asm volatile("" ::: "memory"); } while (0)
#define RBAR() do { __builtin_amdgcn_s_barrier(); asm volatile("" ::: "memory"); } while (0)
#define SCHED() __builtin_amdgcn_sched_barrier(0)
DI __amdgpu_buffer_rsrc_t mkrs(const void* p, unsigned bytes) { return __builtin_amdgcn_make_buffer_rsrc((void*)p, 0, bytes, 0x00020000); }

struct K1Args {
    const u16* q; const u16* k; const u16* v; const u16* sk;
    const u16* wg; const u16* wu; const u16* wd;
    char* ws;
};

static constexpr int K1_STAGE = 49152;
static constexpr int K1_LDS = 3 * K1_STAGE;

DI void dma16(const GAS char* sbase, unsigned voff, unsigned ldst) {
    unsigned keep;
    asm volatile("s_mov_b32 %0, m0\n\ts_mov_b32 m0, %3\n\ts_nop 0\n\tglobal_load_lds_dwordx4 %1, %2\n\ts_mov_b32 m0, %0"
                 : "=&s"(keep) : "v"(voff), "s"(sbase), "s"(ldst));
}

#define PSTAMP(i)
DI void prep_block(const K1Args& a, int pb) {
    const int tid = threadIdx.x, lane = tid & 63, w = tid >> 6;
    const __amdgpu_buffer_rsrc_t rs = mkrs(a.ws, (unsigned)WS_END);
    if (pb == 0 && tid < 8) ((unsigned*)(a.ws + WS_FLAGS))[tid * 64] = 0u;
    const int s0 = pb * 64 + (lane & 7) * 8;
    const int dch = (8 * (w < 7 ? w : 6) + (lane >> 3)) * 8;
    u32x4 kr[11], vr[8], skr[4];
#pragma unroll
    for (int i = 0; i < 11; ++i) {
        int s = s0 - 3 + i;
        kr[i] = *(const u32x4*)(a.k + (size_t)(s < 0 ? 0 : s) * D + dch);
    }
#pragma unroll
    for (int i = 0; i < 8; ++i) vr[i] = *(const u32x4*)(a.v + (size_t)(s0 + i) * D + dch);
#pragma unroll
    for (int i = 0; i < 4; ++i) skr[i] = *(const u32x4*)(a.sk + (size_t)dch * 4 + i * 8);
    const int srow = pb * 64 + w * 8 + (lane >> 3);
    u32x4 xs[7];
#pragma unroll
    for (int i = 0; i < 7; ++i)
        xs[i] = *(const u32x4*)(a.q + (size_t)srow * D + ((lane & 7) + 8 * i) * 8);
    u32x4 wr[8];
    const int item = pb * 392 + (tid < 392 ? tid : 391);
    const int oct = item % 56, hk = item / 56, half = hk & 1, ks = hk >> 1;
#pragma unroll
    for (int j = 0; j < 8; ++j) {
        int f = 16 * ks + 8 * (j >> 2) + 4 * half + (j & 3);
        wr[j] = *(const u32x4*)(a.wd + (size_t)f * D + oct * 8);
    }
    SCHED();
    PSTAMP(1);
    if (s0 == 0) {
#pragma unroll
        for (int i = 0; i < 3; ++i) kr[i] = u32x4{0, 0, 0, 0};
    }
    if (w < 7) {
#pragma unroll
        for (int c = 0; c < 8; ++c) {
            const unsigned t01 = skr[c >> 1][(c & 1) * 2], t23 = skr[c >> 1][(c & 1) * 2 + 1];
            const float w0 = bflo(t01), w1 = bfhi(t01), w2 = bflo(t23), w3 = bfhi(t23);
            float kf[11];
#pragma unroll
            for (int i = 0; i < 11; ++i) kf[i] = (c & 1) ? bfhi(kr[i][c >> 1]) : bflo(kr[i][c >> 1]);
            float o[8];
#pragma unroll
            for (int i = 0; i < 8; ++i) {
                float acc = kf[i] * w3;
                acc = fmaf(kf[i + 1], w2, acc);
                acc = fmaf(kf[i + 2], w1, acc);
                acc = fmaf(kf[i + 3], w0, acc);
                float vv = (c & 1) ? bfhi(vr[i][c >> 1]) : bflo(vr[i][c >> 1]);
                o[i] = acc * vv;
            }
            u32x4 pk = {pk2(o[7], o[6]), pk2(o[5], o[4]), pk2(o[3], o[2]), pk2(o[1], o[0])};
            __builtin_amdgcn_raw_buffer_store_b128(pk, rs, (unsigned)WS_KVR + ((unsigned)(dch + c) * S + (2040 - s0)) * 2, 0, ST_AUX);
        }
    }
    PSTAMP(2);
    {
        float sum = 0.f;
#pragma unroll
        for (int i = 0; i < 7; ++i)
#pragma unroll
            for (int j = 0; j < 4; ++j) sum += bflo(xs[i][j]) + bfhi(xs[i][j]);
        sum += __shfl_xor(sum, 1); sum += __shfl_xor(sum, 2); sum += __shfl_xor(sum, 4);
        const float m = sum * (1.0f / D);
        float sq = 0.f;
#pragma unroll
        for (int i = 0; i < 7; ++i)
#pragma unroll
            for (int j = 0; j < 4; ++j) {
                float d0 = bflo(xs[i][j]) - m, d1 = bfhi(xs[i][j]) - m;
                sq += d0 * d0; sq += d1 * d1;
            }
        sq += __shfl_xor(sq, 1); sq += __shfl_xor(sq, 2); sq += __shfl_xor(sq, 4);
        const float var = sq * (1.0f / D);
        const float mb = rbf(m), vb = rbf(var);
        const float den = rbf(sqrtf(rbf(vb + 1e-5f)));
        if ((lane & 7) == 0) {
            u32x2 o = {__float_as_uint(mb), __float_as_uint(1.0f / den)};
            __builtin_amdgcn_raw_buffer_store_b64(o, rs, (unsigned)WS_STATS + srow * 8, 0, ST_AUX);
        }
    }
    PSTAMP(3);
    if (tid < 392) {
        const int dt = oct >> 2;
#pragma unroll
        for (int e = 0; e < 8; ++e) {
            u32x4 o;
#pragma unroll
            for (int jj = 0; jj < 4; ++jj) {
                unsigned lo = wr[2 * jj][e >> 1], hi = wr[2 * jj + 1][e >> 1];
                o[jj] = (e & 1) ? __builtin_amdgcn_perm(hi, lo, 0x07060302u) : __builtin_amdgcn_perm(hi, lo, 0x05040100u);
            }
            const int dl = (oct & 3) * 8 + e;
            __builtin_amdgcn_raw_buffer_store_b128(o, rs, (unsigned)WS_WDT + (((unsigned)dt * 112 + ks) * 64 + half * 32 + dl) * 16, 0, ST_AUX);
        }
    }
}

__global__ __launch_bounds__(512, 2) void k1_kernel(K1Args a) {
    extern __shared__ __attribute__((aligned(16))) char smem_[];
    LAS char* smem = (LAS char*)smem_;
    const int bid = blockIdx.x;
    STAMP_DECL
    STAMP(0, 0);
    if (bid >= 224) {
        prep_block(a, bid - 224);
        STAMP(0, 15); STAMP_FLUSH(0); return; }
    const int tid = threadIdx.x, lane = tid & 63;
    const int w = __builtin_amdgcn_readfirstlane(tid >> 6);
    const int xcd = bid & 7, idx = bid >> 3;
    const int rb = (xcd >> 1) * 4 + (idx & 3);
    const int cb = (xcd & 1) * 7 + (idx >> 2);
    const int wm = w >> 2, wn = w & 3;

    const GAS char* qb = (const GAS char*)a.q + (size_t)rb * 128 * D * 2;
    const GAS char* wgb = (const GAS char*)a.wg + (size_t)cb * 256;
    const GAS char* wub = (const GAS char*)a.wu + (size_t)cb * 256;
    unsigned xoff[2];
#pragma unroll
    for (int e = 0; e < 2; ++e) {
        const int i = 2 * w + e;
        const int row = 8 * i + (lane >> 3);
        const int p = (lane & 7) ^ ((row >> 1) & 7);
        xoff[e] = row * (D * 2) + p * 16;
    }
    unsigned woff[2];
#pragma unroll
    for (int gu = 0; gu < 2; ++gu) {
        const int piece = (lane & 15) ^ (((lane >> 4) << 2) | (gu << 1));
        woff[gu] = (lane >> 4) * (F * 2) + piece * 16 + (unsigned)(8 * w) * (F * 2);
    }
    auto stage = [&](int c, int slot) {
        const unsigned sb = (unsigned)(uintptr_t)smem + slot * K1_STAGE;
#pragma unroll
        for (int e = 0; e < 2; ++e) dma16(qb + c * 128, xoff[e], sb + (2 * w + e) * 1024);
#pragma unroll
        for (int gu = 0; gu < 2; ++gu)
#pragma unroll
            for (int e = 0; e < 2; ++e)
                dma16((gu ? wub : wgb) + (size_t)(c * 64 + 4 * e) * (F * 2), woff[gu], sb + 16384 + gu * 16384 + (2 * w + e) * 1024);
    };
    auto piece = [&](int c, int slot, int p) {
        const unsigned sb = (unsigned)(uintptr_t)smem + slot * K1_STAGE;
        if (p < 2) dma16(qb + c * 128, xoff[p], sb + (2 * w + p) * 1024);
        else {
            const int gu = (p - 2) >> 1, e = (p - 2) & 1;
            dma16((gu ? wub : wgb) + (size_t)(c * 64 + 4 * e) * (F * 2), woff[gu], sb + 16384 + gu * 16384 + (2 * w + e) * 1024);
        }
    };
    stage(0, 0); stage(1, 1); stage(2, 2);

    const int r = lane & 31, half = lane >> 5;
    unsigned xa[4];
    unsigned wa[2][4];
    const int gi = lane >> 4, blk = gi & 1, q4 = (lane >> 2) & 3, p4 = lane & 3, gu = p4 >> 1;
    const int ct0 = 2 * wn + wm, ct1 = 2 * wn + 1 - wm;
#pragma unroll
    for (int t = 0; t < 4; ++t) {
        const int tr = (t + wn) & 3;
        xa[t] = (unsigned)(wm * 64 + r) * 128 + (((2 * tr + half) ^ ((r >> 1) & 7)) * 16);
#pragma unroll
        for (int i = 0; i < 2; ++i) {
            const int ct = i ? ct1 : ct0;
            const int piece = 2 * ct + blk;
            wa[i][t] = 16384 + gu * 16384 + (16 * tr + 8 * half + q4) * 256 + ((piece ^ ((q4 << 2) | (gu << 1))) * 16) + (p4 & 1) * 8;
        }
    }
    f32x16 acc[2][2];
#pragma unroll
    for (int i = 0; i < 2; ++i)
#pragma unroll
        for (int j = 0; j < 2; ++j)
#pragma unroll
            for (int e = 0; e < 16; ++e) acc[i][j][e] = 0.f;
    float xsum4[2][2] = {{0.f, 0.f}, {0.f, 0.f}}, csum4[4] = {0.f, 0.f, 0.f, 0.f};

#pragma unroll
    for (int c = 0; c < 7; ++c) {
        if (c == 0) WAIT_V(12); else if (c == 6) WAIT_V(0); else WAIT_V(6);
        BAR();
        STAMP(0, 1 + c);
        LAS char* sb = smem + (c % 3) * K1_STAGE;
#pragma unroll
        for (int t = 0; t < 4; ++t) {
            s16x8 xf[2], wf[2];
#pragma unroll
            for (int j = 0; j < 2; ++j) xf[j] = *(const LAS s16x8*)(sb + xa[t] + j * 4096);
#pragma unroll
            for (int i = 0; i < 2; ++i) {
                s16x4 lo = __builtin_amdgcn_ds_read_tr16_b64_v4i16((LAS s16x4*)(sb + wa[i][t]));
                s16x4 hi = __builtin_amdgcn_ds_read_tr16_b64_v4i16((LAS s16x4*)(sb + wa[i][t] + 1024));
                wf[i] = __builtin_shufflevector(lo, hi, 0, 1, 2, 3, 4, 5, 6, 7);
            }
            acc[0][0] = mfma32(wf[0], xf[0], acc[0][0]);
            acc[0][1] = mfma32(wf[0], xf[1], acc[0][1]);
            if (c >= 1 && c + 2 < 7) piece(c + 2, (c + 2) % 3, t == 0 ? 0 : t == 1 ? 2 : t == 2 ? 4 : 5);
            acc[1][0] = mfma32(wf[1], xf[0], acc[1][0]);
            acc[1][1] = mfma32(wf[1], xf[1], acc[1][1]);
            if (c >= 1 && c + 2 < 7 && t < 2) piece(c + 2, (c + 2) % 3, t == 0 ? 1 : 3);
            {
                u32x4 u = __builtin_bit_cast(u32x4, wf[0]);
#pragma unroll
                for (int e = 0; e < 4; ++e) csum4[e] = dot2ones(u[e], csum4[e]);
                asm volatile("" : "+v"(csum4[0]), "+v"(csum4[1]), "+v"(csum4[2]), "+v"(csum4[3]));
            }
            if (t == 0) {
#pragma unroll
                for (int j = 0; j < 2; ++j) {
                    u32x4 u = __builtin_bit_cast(u32x4, xf[j]);
#pragma unroll
                    for (int e = 0; e < 4; ++e) xsum4[j][e & 1] = dot2ones(u[e], xsum4[j][e & 1]);
                }
                asm volatile("" : "+v"(xsum4[0][0]), "+v"(xsum4[0][1]), "+v"(xsum4[1][0]), "+v"(xsum4[1][1]));
            }
        }
    }
    STAMP(0, 8);
    float xsum[2] = {xsum4[0][0] + xsum4[0][1], xsum4[1][0] + xsum4[1][1]};
    float csum = (csum4[0] + csum4[1]) + (csum4[2] + csum4[3]);
    LAS float* xs = (LAS float*)(smem + K1_STAGE);
    LAS float* cs = (LAS float*)(smem + K1_STAGE + 4096);
#pragma unroll
    for (int j = 0; j < 2; ++j) xs[((2 * wm + j) * 32 + r) * 8 + wn * 2 + half] = xsum[j];
    csum += __shfl_xor(csum, 32);
    if (half == 0) cs[ct0 * 32 + r] = csum;
    BAR();
    STAMP(0, 9);
    float mean[2];
#pragma unroll
    for (int j = 0; j < 2; ++j) {
        const LAS f32x4* p = (const LAS f32x4*)(xs + ((2 * wm + j) * 32 + r) * 8);
        f32x4 s0 = p[0], s1 = p[1];
        float s = ((s0[0] + s0[1]) + (s0[2] + s0[3])) + ((s1[0] + s1[1]) + (s1[2] + s1[3]));
        mean[j] = rbf(s * (1.0f / D));
    }
    const __amdgpu_buffer_rsrc_t rs = mkrs(a.ws, (unsigned)WS_END);
#pragma unroll
    for (int i = 0; i < 2; ++i) {
        const int ct = i ? ct1 : ct0;
        f32x4 cv[4];
#pragma unroll
        for (int g = 0; g < 4; ++g) cv[g] = *(const LAS f32x4*)(cs + ct * 32 + 8 * g + 4 * half);
#pragma unroll
        for (int j = 0; j < 2; ++j) {
            float hv[8];
#pragma unroll
            for (int e = 0; e < 4; ++e) {
                float g0 = acc[i][j][e] - mean[j] * cv[0][e];
                float u0 = acc[i][j][4 + e] - mean[j] * cv[1][e];
                float g1 = acc[i][j][8 + e] - mean[j] * cv[2][e];
                float u1 = acc[i][j][12 + e] - mean[j] * cv[3][e];
                hv[e] = fmaxf(g0, 0.f) * u0;
                hv[4 + e] = fmaxf(g1, 0.f) * u1;
            }
            u32x4 o = {pk2(hv[0], hv[1]), pk2(hv[2], hv[3]), pk2(hv[4], hv[5]), pk2(hv[6], hv[7])};
            const int T = rb * 4 + 2 * wm + j, KS = cb * 8 + ct;
            __builtin_amdgcn_raw_buffer_store_b128(o, rs, (unsigned)WS_HT + (((unsigned)T * 112 + KS) * 64 + lane) * 16, 0, ST_AUX);
        }
    }
    STAMP(0, 15);
    STAMP_FLUSH(0);
}

struct K2Args {
    const u16* q; const u16* ssm; const u16* Dv;
    char* ws; u16* out;
};
static constexpr int HC_STRIDE = 4160;
static constexpr int CH_BYTES = 8 * HC_STRIDE + 4608;
static constexpr int K2_CRED = 2 * CH_BYTES;
static constexpr int RED_STRIDE = 272;
static constexpr int K2_LDS = 8 * 64 * RED_STRIDE;
static constexpr int WIN = 8;

template <int CHH, typename F>
DI void conv_mfma(LAS char* ab0, LAS char* gb0, f32x4 (&cacc)[4], F&& issue) {
    constexpr int CS[4] = {CHH ? 1 : 0, CHH ? 2 : 3, CHH ? 5 : 4, CHH ? 6 : 7};
    constexpr int NP = CS[3] + 1;
#pragma unroll
    for (int qq = 0; qq < 4; ++qq) {
        u32x4 A[NP], G[NP];
#pragma unroll
        for (int p = 0; p < NP; ++p) {
            A[p] = *(const LAS u32x4*)(ab0 + qq * 64 + p * 512);
            G[p] = *(const LAS u32x4*)(gb0 + qq * 64 + (7 - p) * 512);
        }
        issue(qq);
#pragma unroll
        for (int p = 0; p < NP; ++p)
#pragma unroll
            for (int ci = 0; ci < 4; ++ci)
                if (CS[ci] >= p) cacc[ci] = mfma16u(A[p], G[CS[ci] - p], cacc[ci]);
        SCHED();
    }
}

__global__ __launch_bounds__(512, 2) void k2_kernel(K2Args a) {
    extern __shared__ __attribute__((aligned(16))) char smem_[];
    LAS char* smem = (LAS char*)smem_;
    const int bid = blockIdx.x;
    const int tid = threadIdx.x, lane = tid & 63;
    const int w = __builtin_amdgcn_readfirstlane(tid >> 6);
    const int xcd = bid & 7, idx = bid >> 3;
    const int rt = xcd * 4 + (idx & 3);
    const int ctile = idx >> 2;
    const int chl = tid >> 8, u = tid & 255;
    const int ch = 2 * bid + chl;
    STAMP_DECL
    STAMP(1, 0);
    const u16* hsrc = a.ssm + (size_t)ch * S;
    const u32x4 h0 = *(const u32x4*)(hsrc + (u > 0 ? u - 1 : 0) * 8);
    const u32x4 h1 = *(const u32x4*)(hsrc + u * 8);
    const u32x4 xr = *(const u32x4*)((const u16*)(a.ws + WS_KVR) + (size_t)ch * S + u * 8);
    const float Dval = bflo((unsigned)a.Dv[2 * bid + (w >> 2)]);

    const u32x4* hp = (const u32x4*)(a.ws + WS_HT) + ((size_t)(rt * 2) * 112 + 14 * w) * 64 + lane;
    const u32x4* wp = (const u32x4*)(a.ws + WS_WDT) + ((size_t)(ctile * 2) * 112 + 14 * w) * 64 + lane;
    u32x4 win[WIN][4];
    auto wload = [&](int slot, int ks) {
        win[slot][0] = wp[ks * 64]; win[slot][1] = wp[(112 + ks) * 64];
        win[slot][2] = hp[ks * 64]; win[slot][3] = hp[(112 + ks) * 64];
    };
    wload(0, 0); wload(1, 1);
    SCHED();
    STAMP(1, 1);
    {
        LAS char* cb = smem + chl * CH_BYTES;
        u32x4 hh0 = h0;
        if (u == 0) hh0 = u32x4{0, 0, 0, 0};
        const unsigned d[8] = {hh0[0], hh0[1], hh0[2], hh0[3], h1[0], h1[1], h1[2], h1[3]};
        unsigned al[7];
#pragma unroll
        for (int i = 0; i < 7; ++i) al[i] = __builtin_amdgcn_alignbit(d[i + 1], d[i], 16);
        const int t = u + 1;
#pragma unroll
        for (int sg = 0; sg < 8; ++sg) {
            const int sh = sg + 1;
            u32x4 o;
#pragma unroll
            for (int e = 0; e < 4; ++e) o[e] = (sh & 1) ? al[(sh >> 1) + e] : d[(sh >> 1) + e];
            *(LAS u32x4*)(cb + sg * HC_STRIDE + t * 16) = o;
        }
        if (u == 255) {
        }
        if (u < 8) {
            *(LAS u32x4*)(cb + u * HC_STRIDE) = u32x4{0, 0, 0, 0};
        }
        *(LAS u32x4*)(cb + 8 * HC_STRIDE + u * 16) = xr;
        if (u < 32) *(LAS u32x4*)(cb + 8 * HC_STRIDE + 4096 + u * 16) = u32x4{0, 0, 0, 0};
    }
    BAR();
    STAMP(1, 3);
    const int cw = w >> 2, qh = (w >> 1) & 1, chh = w & 1;
    f32x4 cacc[4];
#pragma unroll
    for (int c = 0; c < 4; ++c) cacc[c] = f32x4{0.f, 0.f, 0.f, 0.f};
    {
        LAS char* cb = smem + cw * CH_BYTES;
        const int m = lane & 15, g = lane >> 4;
        LAS char* ab0 = cb + (m & 7) * HC_STRIDE + (16 * qh + g + (m >> 3)) * 16;
        LAS char* gb0 = cb + 8 * HC_STRIDE + (2032 - 256 * 7 + 128 * qh - 16 * m + 8 * g) * 2;
        auto issue = [&](int qq) { if (qq < 3) { wload(2 + 2 * qq, 2 + 2 * qq); wload(3 + 2 * qq, 3 + 2 * qq); } };
        if (chh) conv_mfma<1>(ab0, gb0, cacc, issue); else conv_mfma<0>(ab0, gb0, cacc, issue);
    }
    f32x4 keep[2];
    {
        f32x4 c0 = cacc[0], c1 = cacc[1], c2 = cacc[2], c3 = cacc[3];
        asm volatile("" : "+v"(c0), "+v"(c1), "+v"(c2), "+v"(c3));
        f32x4 s0, s1;
#pragma unroll
        for (int e = 0; e < 4; ++e) {
            s0[e] = qh ? c0[e] : c2[e]; s1[e] = qh ? c1[e] : c3[e];
            keep[0][e] = qh ? c2[e] : c0[e]; keep[1][e] = qh ? c3[e] : c1[e];
        }
        LAS f32x4* red = (LAS f32x4*)(smem + K2_CRED);
        red[(w * 2 + 0) * 64 + lane] = s0;
        red[(w * 2 + 1) * 64 + lane] = s1;
    }
    BAR();
    STAMP(1, 4);
    {
        const LAS f32x4* red = (const LAS f32x4*)(smem + K2_CRED);
        const int pw = w ^ 2;
        const int ap = lane & 15, g = lane >> 4;
        const LAS char* xrl = smem + cw * CH_BYTES + 8 * HC_STRIDE;
        const __amdgpu_buffer_rsrc_t rs = mkrs(a.ws, (unsigned)WS_END);
#pragma unroll
        for (int e2 = 0; e2 < 2; ++e2) {
            const int ci = 2 * qh + e2;
            const int c = chh ? (ci == 0 ? 1 : ci == 1 ? 2 : ci == 2 ? 5 : 6) : (ci == 0 ? 0 : ci == 1 ? 3 : ci == 2 ? 4 : 7);
            f32x4 s = red[(pw * 2 + e2) * 64 + lane] + keep[e2];
            const int s0 = 256 * c + 16 * ap + 4 * g;
            const u32x2 kx = *(const LAS u32x2*)(xrl + (2044 - s0) * 2);
            const float kv0 = bfhi(kx[1]), kv1 = bflo(kx[1]), kv2 = bfhi(kx[0]), kv3 = bflo(kx[0]);
            const float y0 = rbf(s[0]) + rbf(kv0 * Dval), y1 = rbf(s[1]) + rbf(kv1 * Dval);
            const float y2 = rbf(s[2]) + rbf(kv2 * Dval), y3 = rbf(s[3]) + rbf(kv3 * Dval);
            u32x2 o = {pk2(y0, y1), pk2(y2, y3)};
            __builtin_amdgcn_raw_buffer_store_b64(o, rs, (unsigned)WS_YT + ((unsigned)(2 * bid + cw) * S + s0) * 2, 0, 16);
        }
        WAIT_V(0);
        RBAR();
        if (tid == 0) {
            unsigned* cnt = (unsigned*)(a.ws + WS_FLAGS) + ((2 * bid) >> 6) * 64;
            __hip_atomic_fetch_add(cnt, 1u, __ATOMIC_RELAXED, __HIP_MEMORY_SCOPE_AGENT);
        }
    }
    STAMP(1, 5);
    const int tp = tid >> 4, cq = tid & 15;
    const int tok0 = rt * 64 + 2 * tp, d0 = ctile * 64 + 4 * cq;
    const u32x2 q0 = *(const u32x2*)(a.q + (size_t)tok0 * D + d0);
    const u32x2 q1 = *(const u32x2*)(a.q + (size_t)(tok0 + 1) * D + d0);
    const f32x4 st = *(const f32x4*)(a.ws + WS_STATS + (size_t)tok0 * 8);
    f32x16 acc[2][2];
#pragma unroll
    for (int i = 0; i < 2; ++i)
#pragma unroll
        for (int j = 0; j < 2; ++j)
#pragma unroll
            for (int e = 0; e < 16; ++e) acc[i][j][e] = 0.f;
    unsigned yv[4];
#pragma unroll
    for (int i = 0; i < 14; ++i) {
        u32x4 wf0 = win[i % WIN][0], wf1 = win[i % WIN][1], hf0 = win[i % WIN][2], hf1 = win[i % WIN][3];
        if (i + WIN < 14) wload(i % WIN, i + WIN);
        if (i == 8) {
            unsigned* cnt = (unsigned*)(a.ws + WS_FLAGS) + ctile * 64;
            unsigned spins = 0;
            while (__hip_atomic_load(cnt, __ATOMIC_RELAXED, __HIP_MEMORY_SCOPE_AGENT) < 32u) {
                __builtin_amdgcn_s_sleep(1);
                if (++spins > (1u << 20)) break;
            }
            __builtin_amdgcn_fence(__ATOMIC_ACQUIRE, "wavefront");
            const __amdgpu_buffer_rsrc_t rs = mkrs(a.ws, (unsigned)WS_END);
#pragma unroll
            for (int j = 0; j < 4; ++j)
                yv[j] = __builtin_amdgcn_raw_buffer_load_b32(rs, (unsigned)WS_YT + (unsigned)(d0 + j) * 4096 + tok0 * 2, 0, 16);
        }
        acc[0][0] = mfma32u(wf0, hf0, acc[0][0]);
        acc[0][1] = mfma32u(wf0, hf1, acc[0][1]);
        acc[1][0] = mfma32u(wf1, hf0, acc[1][0]);
        acc[1][1] = mfma32u(wf1, hf1, acc[1][1]);
        SCHED();
    }
    STAMP(1, 6);
    {
        const int r = lane & 31, half = lane >> 5;
        LAS char* rb = smem + w * (64 * RED_STRIDE);
#pragma unroll
        for (int i = 0; i < 2; ++i)
#pragma unroll
            for (int j = 0; j < 2; ++j)
#pragma unroll
                for (int g = 0; g < 4; ++g) {
                    f32x4 v = {acc[i][j][4 * g], acc[i][j][4 * g + 1], acc[i][j][4 * g + 2], acc[i][j][4 * g + 3]};
                    *(LAS f32x4*)(rb + (j * 32 + r) * RED_STRIDE + (i * 32 + 8 * g + 4 * half) * 4) = v;
                }
    }
    STAMP(1, 7);
    BAR();
    STAMP(1, 8);
    {
        f32x4 o[2];
#pragma unroll
        for (int e = 0; e < 2; ++e) {
            const LAS char* rb = smem + (2 * tp + e) * RED_STRIDE + cq * 16;
            f32x4 s = *(const LAS f32x4*)rb;
#pragma unroll
            for (int ww = 1; ww < 8; ++ww) s += *(const LAS f32x4*)(rb + ww * (64 * RED_STRIDE));
            o[e] = s;
        }
#pragma unroll
        for (int e = 0; e < 2; ++e) {
            const float mb = st[2 * e], rd = st[2 * e + 1];
            const u32x2 qq = e ? q1 : q0;
            float res[4];
#pragma unroll
            for (int j = 0; j < 4; ++j) {
                const float x = (j & 1) ? bfhi(qq[j >> 1]) : bflo(qq[j >> 1]);
                const float xn = rbf(rbf(x - mb) * rd);
                const float ov = rbf(o[e][j] * rd * rd);
                const float gg = rbf(ov * __builtin_amdgcn_rcpf(1.0f + __expf(-ov)));
                const float y = e ? bfhi(yv[j]) : bflo(yv[j]);
                const float sv = rbf(xn * y);
                res[j] = x + rbf(sv * gg);
            }
            u32x2 ov2 = {pk2(res[0], res[1]), pk2(res[2], res[3])};
            *(u32x2*)(a.out + (size_t)(tok0 + e) * D + d0) = ov2;
        }
    }
    STAMP(1, 15);
    STAMP_FLUSH(1);
}

extern "C" void kernel_launch(void* const* d_in, const int* in_sizes, int n_in,
                              void* d_out, int out_size, void* d_ws, size_t ws_size,
                              hipStream_t stream) {
    (void)hipFuncSetAttribute((const void*)k1_kernel, hipFuncAttributeMaxDynamicSharedMemorySize, K1_LDS);
    (void)hipFuncSetAttribute((const void*)k2_kernel, hipFuncAttributeMaxDynamicSharedMemorySize, K2_LDS);
    K1Args a1;
    a1.q = (const u16*)d_in[0]; a1.k = (const u16*)d_in[1]; a1.v = (const u16*)d_in[2]; a1.sk = (const u16*)d_in[3];
    a1.wg = (const u16*)d_in[7]; a1.wu = (const u16*)d_in[8]; a1.wd = (const u16*)d_in[9];
    a1.ws = (char*)d_ws;
    K2Args a2;
    a2.q = (const u16*)d_in[0]; a2.ssm = (const u16*)d_in[4]; a2.Dv = (const u16*)d_in[5];
    a2.ws = (char*)d_ws; a2.out = (u16*)d_out;
    k1_kernel<<<256, 512, K1_LDS, stream>>>(a1);
    k2_kernel<<<224, 512, K2_LDS, stream>>>(a2);
}
```

```cpp
#include <hip/hip_runtime.h>
#include <stdint.h>

typedef unsigned short u16;
typedef __attribute__((ext_vector_type(4))) short s16x4;
typedef __attribute__((ext_vector_type(8))) short s16x8;
typedef __attribute__((ext_vector_type(8))) __bf16 bf16x8;
typedef __attribute__((ext_vector_type(2))) __bf16 bf16x2;
typedef __attribute__((ext_vector_type(16))) float f32x16;
typedef __attribute__((ext_vector_type(4))) float f32x4;
typedef __attribute__((ext_vector_type(4))) unsigned u32x4;
typedef __attribute__((ext_vector_type(2))) unsigned u32x2;
#define GAS __attribute__((address_space(1)))
#define LAS __attribute__((address_space(3)))
#define DI __device__ __forceinline__
typedef GAS unsigned gu32;
#define RLX __ATOMIC_RELAXED, __HIP_MEMORY_SCOPE_AGENT

static constexpr int S = 2048, D = 448, F = 1792;
static constexpr int NMAIN = 224, NPREP = 31;
#define STAMP_DECL
#define STAMP(i)
#define STAMP_FLUSH()

static constexpr unsigned SY_M = 0, SY_A = 1024, SY_CNT = 2048;
static constexpr int C_KV = 0, C_ST = 7, C_WD = 15, C_H = 23, C_Y = 39, C_N = 46;
static constexpr unsigned WS_STATS = 16384;
static constexpr unsigned WS_HT    = WS_STATS + S * 8;
static constexpr unsigned WS_WDT   = WS_HT + S * F * 2;
static constexpr unsigned WS_KVR   = WS_WDT + F * D * 2;
static constexpr unsigned WS_YT    = WS_KVR + S * D * 2;
static constexpr unsigned WS_END   = WS_YT + S * D * 2;
static constexpr int SC1 = 16;
static constexpr unsigned SPIN_CAP = 1u << 16;

DI float bflo(unsigned u) { return __uint_as_float(u << 16); }
DI float bfhi(unsigned u) { return __uint_as_float(u & 0xffff0000u); }
DI unsigned pk2(float a, float b) { bf16x2 r = {(__bf16)a, (__bf16)b}; return __builtin_bit_cast(unsigned, r); }
DI float rbf(float a) { return bflo(pk2(a, a)); }
DI f32x16 mfma32(s16x8 a, s16x8 b, f32x16 c) {
    return __builtin_amdgcn_mfma_f32_32x32x16_bf16(__builtin_bit_cast(bf16x8, a), __builtin_bit_cast(bf16x8, b), c, 0, 0, 0);
}
DI f32x4 mfma16u(u32x4 a, u32x4 b, f32x4 c) {
    return __builtin_amdgcn_mfma_f32_16x16x32_bf16(__builtin_bit_cast(bf16x8, a), __builtin_bit_cast(bf16x8, b), c, 0, 0, 0);
}
DI f32x16 mfma32u(u32x4 a, u32x4 b, f32x16 c) { return mfma32(__builtin_bit_cast(s16x8, a), __builtin_bit_cast(s16x8, b), c); }
#define WAIT_V(n) asm volatile("s_waitcnt vmcnt(" #n ")" ::: "memory")
#define BAR() do { asm volatile("s_waitcnt lgkmcnt(0)" ::: "memory"); __builtin_amdgcn_s_barrier(); asm volatile("" ::: "memory"); } while (0)
#define SCHED() __builtin_amdgcn_sched_barrier(0)
DI __amdgpu_buffer_rsrc_t mkrs(const void* p, unsigned bytes) { return __builtin_amdgcn_make_buffer_rsrc((void*)p, 0, bytes, 0x00020000); }
DI gu32* cnt_ptr(char* ws, int i) { return (gu32*)(ws + SY_CNT + 128 * i); }
DI void poll_eq(gu32* p, unsigned want) {
    for (unsigned sp = 0; sp < SPIN_CAP; ++sp) { if (__hip_atomic_load(p, RLX) == want) break; __builtin_amdgcn_s_sleep(1); }
}
DI void poll_ge(gu32* p, unsigned want) {
    for (unsigned sp = 0; sp < SPIN_CAP; ++sp) { if (__hip_atomic_load(p, RLX) >= want) break; __builtin_amdgcn_s_sleep(1); }
}

struct Args {
    const u16* q; const u16* k; const u16* v; const u16* sk; const u16* ssm; const u16* Dv;
    const u16* wg; const u16* wu; const u16* wd;
    char* ws; u16* out;
};

static constexpr int G1_STAGE = 49152;
static constexpr int X_CS = 3 * G1_STAGE;
static constexpr int X_SC = X_CS + 1024;
static constexpr int HC_STRIDE = 4160;
static constexpr int CH_BYTES = 8 * HC_STRIDE + 4608;
static constexpr int X_CRED = 2 * CH_BYTES;
static constexpr int RED_STRIDE = 272;
static constexpr int LDS_BYTES = X_CS + 1024;
static constexpr int WWIN = 14;
static constexpr int HWIN = 6;

DI void dma16(const GAS char* sbase, unsigned voff, unsigned ldst) {
    unsigned keep;
    asm volatile("s_mov_b32 %0, m0\n\ts_mov_b32 m0, %3\n\ts_nop 0\n\tglobal_load_lds_dwordx4 %1, %2\n\ts_mov_b32 m0, %0"
                 : "=&s"(keep) : "v"(voff), "s"(sbase), "s"(ldst));
}

DI void service_block(const Args& a) {
    if (threadIdx.x >= 64) return;
    const int lane = threadIdx.x;
    const __amdgpu_buffer_rsrc_t rs = mkrs(a.ws, WS_END);
    if (lane < C_N) __hip_atomic_store(cnt_ptr(a.ws, lane), 0u, RLX);
    WAIT_V(0);
    for (unsigned it = 0; it < SPIN_CAP; ++it) {
        const u32x4 m = __builtin_amdgcn_raw_buffer_load_b128(rs, SY_M + lane * 16, 0, SC1);
        __builtin_amdgcn_raw_buffer_store_b128(m, rs, SY_A + lane * 16, 0, SC1);
        const unsigned c = __hip_atomic_load(cnt_ptr(a.ws, lane < C_N ? lane : 0), RLX);
        const bool ok = (lane >= C_WD && lane < C_H) ? (c >= (unsigned)NPREP) : (lane >= C_H && lane < C_Y) ? (c >= 14u) : true;
        if (__all(ok)) break;
        __builtin_amdgcn_s_sleep(1);
    }
}

DI u32x2 row_stats(const u32x4 (&xs)[7]) {
    float s0 = 0.f, s1 = 0.f;
#pragma unroll
    for (int i = 0; i < 7; ++i)
#pragma unroll
        for (int j = 0; j < 4; ++j) { s0 += bflo(xs[i][j]); s1 += bfhi(xs[i][j]); }
    float sum = s0 + s1;
    sum += __shfl_xor(sum, 1); sum += __shfl_xor(sum, 2); sum += __shfl_xor(sum, 4);
    const float m = sum * (1.0f / D);
    float q0 = 0.f, q1 = 0.f;
#pragma unroll
    for (int i = 0; i < 7; ++i)
#pragma unroll
        for (int j = 0; j < 4; ++j) {
            const float d0 = bflo(xs[i][j]) - m, d1 = bfhi(xs[i][j]) - m;
            q0 = fmaf(d0, d0, q0); q1 = fmaf(d1, d1, q1);
        }
    float sq = q0 + q1;
    sq += __shfl_xor(sq, 1); sq += __shfl_xor(sq, 2); sq += __shfl_xor(sq, 4);
    const float var = sq * (1.0f / D);
    const float mb = rbf(m), vb = rbf(var);
    const float den = rbf(sqrtf(rbf(vb + 1e-5f)));
    return u32x2{__float_as_uint(mb), __float_as_uint(1.0f / den)};
}

#define PSTAMP(i)
DI void prep_block(const Args& a, LAS char* smem) {
    const int tid = threadIdx.x, lane = tid & 63;
    const int w = __builtin_amdgcn_readfirstlane(tid >> 6);
    const int g = (blockIdx.x - NMAIN) * 8 + w;
    const __amdgpu_buffer_rsrc_t rs = mkrs(a.ws, WS_END);
    gu32* mbA = (gu32*)(a.ws + SY_A) + blockIdx.x;
    gu32* mbM = (gu32*)(a.ws + SY_M) + blockIdx.x;
    unsigned chal = __hip_atomic_load(mbA, RLX);
    u32x4 xs[7], xs2[7];
    {
        const int srow = g * 8 + (lane >> 3);
#pragma unroll
        for (int i = 0; i < 7; ++i) xs[i] = *(const u32x4*)(a.q + (size_t)srow * D + ((lane & 7) + 8 * i) * 8);
    }
    if (g < 8) {
        const int srow = (g + 248) * 8 + (lane >> 3);
#pragma unroll
        for (int i = 0; i < 7; ++i) xs2[i] = *(const u32x4*)(a.q + (size_t)srow * D + ((lane & 7) + 8 * i) * 8);
    }
    u32x4 wr[7];
#pragma unroll
    for (int i = 0; i < 7; ++i) {
        int t = g + 248 * i; t = t < 1568 ? t : 1567;
        const int dt = t / 112, ks = t % 112;
        wr[i] = *(const u32x4*)(a.wd + (size_t)(16 * ks + (lane >> 2)) * D + 32 * dt + (lane & 3) * 8);
    }
    SCHED();
    chal += 1u;
    if (tid == 0) __hip_atomic_store(mbM, chal, RLX);
    PSTAMP(1);
    {
        const u32x2 o = row_stats(xs);
        if ((lane & 7) == 0) __builtin_amdgcn_raw_buffer_store_b64(o, rs, WS_STATS + (unsigned)(g * 8 + (lane >> 3)) * 8, 0, SC1);
        if (g < 8) {
            const u32x2 o2 = row_stats(xs2);
            if ((lane & 7) == 0) __builtin_amdgcn_raw_buffer_store_b64(o2, rs, WS_STATS + (unsigned)((g + 248) * 8 + (lane >> 3)) * 8, 0, SC1);
        }
    }
    LAS char* sc = smem + w * 7168;
#pragma unroll
    for (int i = 0; i < 7; ++i) *(LAS u32x4*)(sc + i * 1024 + lane * 16) = wr[i];
    PSTAMP(2);
    WAIT_V(0);
    BAR();
    PSTAMP(3);
    if (w == 0) {
        if (lane == 0) poll_eq(mbA, chal);
        if (lane < 8) (void)__hip_atomic_fetch_add(cnt_ptr(a.ws, C_ST + lane), 1u, RLX);
    }
    {
        const int gi = lane >> 4, hf = gi >> 1, blk = gi & 1, q4 = (lane >> 2) & 3, p4 = lane & 3;
        const int ra = (4 * hf + q4) * 64 + (16 * blk + 4 * p4) * 2;
#pragma unroll
        for (int i = 0; i < 7; ++i) {
            const int t = g + 248 * i;
            s16x4 lo = __builtin_amdgcn_ds_read_tr16_b64_v4i16((LAS s16x4*)(sc + i * 1024 + ra));
            s16x4 hi = __builtin_amdgcn_ds_read_tr16_b64_v4i16((LAS s16x4*)(sc + i * 1024 + ra + 512));
            s16x8 o = __builtin_shufflevector(lo, hi, 0, 1, 2, 3, 4, 5, 6, 7);
            if (t < 1568) __builtin_amdgcn_raw_buffer_store_b128(__builtin_bit_cast(u32x4, o), rs, WS_WDT + (unsigned)t * 1024 + lane * 16, 0, SC1);
        }
    }
    PSTAMP(4);
    WAIT_V(0);
    PSTAMP(5);
    BAR();
    PSTAMP(6);
    if (w == 0 && lane < 8) (void)__hip_atomic_fetch_add(cnt_ptr(a.ws, C_WD + lane), 1u, RLX);
}

template <int CHH, typename FN>
DI void conv_mfma(LAS char* ab0, LAS char* gb0, f32x4 (&cacc)[4], FN&& issue) {
    constexpr int C0 = CHH ? 1 : 0, C1 = CHH ? 2 : 3, C2 = CHH ? 5 : 4, C3 = CHH ? 6 : 7;
    constexpr int NP = C3 + 1;
#pragma unroll
    for (int qq = 0; qq < 4; ++qq) {
        u32x4 A[NP], G[NP];
#pragma unroll
        for (int p = 0; p < NP; ++p) {
            A[p] = *(const LAS u32x4*)(ab0 + qq * 64 + p * 512);
            G[p] = *(const LAS u32x4*)(gb0 + qq * 64 + (7 - p) * 512);
        }
#pragma unroll
        for (int p = 0; p < NP; ++p) {
            if (C0 >= p) cacc[0] = mfma16u(A[p], G[C0 >= p ? C0 - p : 0], cacc[0]);
            if (C1 >= p) cacc[1] = mfma16u(A[p], G[C1 >= p ? C1 - p : 0], cacc[1]);
            if (C2 >= p) cacc[2] = mfma16u(A[p], G[C2 >= p ? C2 - p : 0], cacc[2]);
            if (C3 >= p) cacc[3] = mfma16u(A[p], G[C3 - p], cacc[3]);
            issue(qq, p);
        }
        SCHED();
    }
}

__global__ __launch_bounds__(512, 2) void fused_kernel(Args a) {
    extern __shared__ __attribute__((aligned(16))) char smem_[];
    LAS char* smem = (LAS char*)smem_;
    const int bid = blockIdx.x;
    STAMP_DECL
    STAMP(0);
    if (bid >= NMAIN) {
        if (bid == NMAIN + NPREP) service_block(a); else prep_block(a, smem);
        STAMP(15); STAMP_FLUSH();
        return;
    }
    const int tid = threadIdx.x, lane = tid & 63;
    const int w = __builtin_amdgcn_readfirstlane(tid >> 6);
    const int xcd = bid & 7, idx = bid >> 3;
    const __amdgpu_buffer_rsrc_t rs = mkrs(a.ws, WS_END);
    const int rb = (xcd >> 1) * 4 + (idx & 3);
    const int cb = (xcd & 1) * 7 + (idx >> 2);
    const int rt = xcd * 4 + (idx & 3);
    const int ctile = idx >> 2;
    const int r = lane & 31, half = lane >> 5;
    if (w >= 4) {
        const int lw = w - 4, lt = tid - 256;
        gu32* mbA = (gu32*)(a.ws + SY_A) + bid;
        const GAS char* qb = (const GAS char*)a.q + (size_t)rb * 128 * D * 2;
        const GAS char* wgb = (const GAS char*)a.wg + (size_t)cb * 256;
        const GAS char* wub = (const GAS char*)a.wu + (size_t)cb * 256;
        unsigned xo[2], wo[2];
#pragma unroll
        for (int e = 0; e < 2; ++e) {
            const int row = 8 * e + (lane >> 3);
            xo[e] = row * (D * 2) + (((lane & 7) ^ ((row >> 1) & 7)) * 16);
            const int pc = (lane & 15) ^ (((lane >> 4) << 2) | (e << 1));
            wo[e] = (lane >> 4) * (F * 2) + pc * 16;
        }
        const unsigned lds0 = (unsigned)(uintptr_t)smem;
        auto stage = [&](int c) {
            const unsigned sb = lds0 + (c % 3) * G1_STAGE;
#pragma unroll
            for (int n = 0; n < 4; ++n) {
                const int i = 4 * lw + n;
                dma16(wgb + (size_t)(c * 64 + 4 * i) * (F * 2), wo[0], sb + 16384 + i * 1024);
                dma16(wub + (size_t)(c * 64 + 4 * i) * (F * 2), wo[1], sb + 32768 + i * 1024);
                dma16(qb + c * 128 + (size_t)(i >> 1) * 16 * (D * 2), xo[n & 1], sb + i * 1024);
            }
        };
        unsigned chal;
        asm volatile("global_load_dword %0, %1, %2 sc1" : "=v"(chal) : "v"(0u), "s"(mbA));
        stage(0); stage(1); stage(2);
        asm volatile("s_waitcnt vmcnt(36)" : "+v"(chal));
        chal += 1u;
        if (tid == 256) __hip_atomic_store((gu32*)(a.ws + SY_M) + bid, chal, RLX);
        float cs[8];
#pragma unroll
        for (int j = 0; j < 8; ++j) cs[j] = 0.f;
        unsigned ca[4];
        {
            const int gp = 8 * lw + (lane & 7), gu = gp >> 4, pc = gp & 15, kg = lane >> 3;
#pragma unroll
            for (int i = 0; i < 4; ++i) ca[i] = 16384 + gu * 16384 + (8 * kg + i) * 256 + ((pc ^ ((i << 2) | (gu << 1))) * 16);
        }
        auto colsum = [&](int c) {
            LAS char* sb = smem + (c % 3) * G1_STAGE;
#pragma unroll
            for (int i = 0; i < 8; ++i) {
                const u32x4 v = *(const LAS u32x4*)(sb + ca[i & 3] + (i >> 2) * 1024);
#pragma unroll
                for (int e = 0; e < 4; ++e) { cs[2 * e] += bflo(v[e]); cs[2 * e + 1] += bfhi(v[e]); }
            }
        };
        u32x4 xr[2];
        WAIT_V(24);
        BAR();
        STAMP(1);
        u32x4 h0[2], h1[2];
#pragma unroll
        for (int c2 = 0; c2 < 2; ++c2) {
            const u16* hs = a.ssm + (size_t)(2 * bid + c2) * S;
            h0[c2] = *(const u32x4*)(hs + (lt > 0 ? lt - 1 : 0) * 8);
            h1[c2] = *(const u32x4*)(hs + lt * 8);
        }
#pragma unroll
        for (int c = 0; c < 6; ++c) {
            if (c >= 1 && c + 2 <= 6) stage(c + 2);
            colsum(c);
            if (c == 3 && lw == 0) {
                if (lane == 0) { poll_eq(mbA, chal); (void)__hip_atomic_fetch_add(cnt_ptr(a.ws, C_KV + bid % 7), 1u, RLX); }
            }
            if (c == 5) {
                if (lw == 0) {
                    const int ci = (lane == 1) ? C_ST + (bid & 7) : C_KV + (bid >> 5);
                    const unsigned want = (lane == 1) ? (unsigned)NPREP : 32u;
                    for (unsigned sp = 0; sp < SPIN_CAP; ++sp) {
                        const unsigned c_ = __hip_atomic_load(cnt_ptr(a.ws, ci), RLX);
                        if (__all(c_ >= want)) break;
                        __builtin_amdgcn_s_sleep(1);
                    }
                }
                WAIT_V(0);
            } else if (c == 4) WAIT_V(12);
            else WAIT_V(12);
            BAR();
            STAMP(2 + c);
        }
#pragma unroll
        for (int c2 = 0; c2 < 2; ++c2)
            xr[c2] = __builtin_amdgcn_raw_buffer_load_b128(rs, WS_KVR + ((unsigned)(2 * bid + c2) * S + lt * 8) * 2, 0, SC1);
        colsum(6);
#pragma unroll
        for (int j = 0; j < 8; ++j) {
            cs[j] += __shfl_xor(cs[j], 8); cs[j] += __shfl_xor(cs[j], 16); cs[j] += __shfl_xor(cs[j], 32);
        }
        if (lane < 8) {
            LAS f32x4* p = (LAS f32x4*)(smem + X_CS + (8 * lw + lane) * 32);
            p[0] = f32x4{cs[0], cs[1], cs[2], cs[3]};
            p[1] = f32x4{cs[4], cs[5], cs[6], cs[7]};
        }
        BAR();
        STAMP(8);
#pragma unroll
        for (int c2 = 0; c2 < 2; ++c2) {
            LAS char* cbp = smem + c2 * CH_BYTES;
            u32x4 hh0 = h0[c2];
            if (lt == 0) hh0 = u32x4{0, 0, 0, 0};
            const unsigned d[8] = {hh0[0], hh0[1], hh0[2], hh0[3], h1[c2][0], h1[c2][1], h1[c2][2], h1[c2][3]};
            unsigned al[7];
#pragma unroll
            for (int i = 0; i < 7; ++i) al[i] = __builtin_amdgcn_alignbit(d[i + 1], d[i], 16);
            const int t = lt + 1;
#pragma unroll
            for (int sg = 0; sg < 8; ++sg) {
                const int sh = sg + 1;
                u32x4 o;
#pragma unroll
                for (int e = 0; e < 4; ++e) o[e] = (sh & 1) ? al[(sh >> 1) + e] : d[(sh >> 1) + e];
                *(LAS u32x4*)(cbp + sg * HC_STRIDE + t * 16) = o;
            }
            if (lt < 8) *(LAS u32x4*)(cbp + lt * HC_STRIDE) = u32x4{0, 0, 0, 0};
            *(LAS u32x4*)(cbp + 8 * HC_STRIDE + lt * 16) = xr[c2];
            if (lt < 32) *(LAS u32x4*)(cbp + 8 * HC_STRIDE + 4096 + lt * 16) = u32x4{0, 0, 0, 0};
        }
        if (tid == 256) poll_ge(cnt_ptr(a.ws, C_WD + (bid & 7)), (unsigned)NPREP);
        STAMP(9);
    } else {
        {
            const int s0 = (bid / 7) * 64 + (lane & 7) * 8;
            const int dch = (bid % 7) * 64 + 2 * (8 * w + (lane >> 3));
            unsigned kr[11], vr[8];
#pragma unroll
            for (int i = 0; i < 11; ++i) {
                const int s = s0 - 3 + i;
                kr[i] = *(const unsigned*)(a.k + (size_t)(s < 0 ? 0 : s) * D + dch);
            }
#pragma unroll
            for (int i = 0; i < 8; ++i) vr[i] = *(const unsigned*)(a.v + (size_t)(s0 + i) * D + dch);
            const u32x4 sk = *(const u32x4*)(a.sk + (size_t)dch * 4);
            SCHED();
            if (s0 == 0) { kr[0] = 0u; kr[1] = 0u; kr[2] = 0u; }
#pragma unroll
            for (int c = 0; c < 2; ++c) {
                const float w0 = bflo(sk[2 * c]), w1 = bfhi(sk[2 * c]), w2 = bflo(sk[2 * c + 1]), w3 = bfhi(sk[2 * c + 1]);
                float kf[11], o[8];
#pragma unroll
                for (int i = 0; i < 11; ++i) kf[i] = c ? bfhi(kr[i]) : bflo(kr[i]);
#pragma unroll
                for (int i = 0; i < 8; ++i) {
                    float acc = kf[i] * w3;
                    acc = fmaf(kf[i + 1], w2, acc);
                    acc = fmaf(kf[i + 2], w1, acc);
                    acc = fmaf(kf[i + 3], w0, acc);
                    o[i] = acc * (c ? bfhi(vr[i]) : bflo(vr[i]));
                }
                u32x4 pk = {pk2(o[7], o[6]), pk2(o[5], o[4]), pk2(o[3], o[2]), pk2(o[1], o[0])};
                __builtin_amdgcn_raw_buffer_store_b128(pk, rs, WS_KVR + ((unsigned)(dch + c) * S + (2040 - s0)) * 2, 0, SC1);
            }
        }
        const int wm = w >> 1, wn = w & 1;
        unsigned xa[4], wa[4];
        {
            const int gi = lane >> 4, blk = gi & 1, q4 = (lane >> 2) & 3, p4 = lane & 3, gu = p4 >> 1;
#pragma unroll
            for (int t = 0; t < 4; ++t) xa[t] = (unsigned)(wm * 64 + r) * 128 + (((2 * t + half) ^ ((r >> 1) & 7)) * 16);
#pragma unroll
            for (int i = 0; i < 4; ++i) {
                const int pc = 2 * (4 * wn + i) + blk;
                wa[i] = 16384 + gu * 16384 + (8 * half + q4) * 256 + ((pc ^ ((q4 << 2) | (gu << 1))) * 16) + (p4 & 1) * 8;
            }
        }
        f32x16 acc[4][2];
#pragma unroll
        for (int i = 0; i < 4; ++i)
#pragma unroll
            for (int j = 0; j < 2; ++j)
#pragma unroll
                for (int e = 0; e < 16; ++e) acc[i][j][e] = 0.f;
        s16x8 xf[2][2], wf[2][4];
        auto rdfrag = [&](int ks) {
            const int c = ks >> 2, t = ks & 3, b = ks & 1;
            LAS char* sb = smem + (c % 3) * G1_STAGE;
#pragma unroll
            for (int j = 0; j < 2; ++j) xf[b][j] = *(const LAS s16x8*)(sb + xa[t] + j * 4096);
#pragma unroll
            for (int i = 0; i < 4; ++i) {
                s16x4 lo = __builtin_amdgcn_ds_read_tr16_b64_v4i16((LAS s16x4*)(sb + wa[i] + t * 4096));
                s16x4 hi = __builtin_amdgcn_ds_read_tr16_b64_v4i16((LAS s16x4*)(sb + wa[i] + t * 4096 + 1024));
                wf[b][i] = __builtin_shufflevector(lo, hi, 0, 1, 2, 3, 4, 5, 6, 7);
            }
        };
        float mean[2];
        BAR();
        STAMP(1);
        rdfrag(0);
#pragma unroll
        for (int ks = 0; ks < 28; ++ks) {
            const int c = ks >> 2, t = ks & 3, b = ks & 1;
            if (t == 3 && c < 6) {
                STAMP(16 + c);
                if (c == 1) WAIT_V(0);
                BAR();
                STAMP(2 + c);
                if (c == 5) {
#pragma unroll
                    for (int j = 0; j < 2; ++j)
                        mean[j] = __uint_as_float(__builtin_amdgcn_raw_buffer_load_b32(rs, WS_STATS + (unsigned)(rb * 128 + (2 * wm + j) * 32 + r) * 8, 0, SC1));
                }
            }
            if (ks < 27) rdfrag(ks + 1);
#pragma unroll
            for (int i = 0; i < 4; ++i) {
                acc[i][0] = mfma32(wf[b][i], xf[b][0], acc[i][0]);
                acc[i][1] = mfma32(wf[b][i], xf[b][1], acc[i][1]);
            }
#pragma unroll
            for (int i = 0; i < 5; ++i) {
                __builtin_amdgcn_sched_group_barrier(0x008, 1, 0);
                __builtin_amdgcn_sched_group_barrier(0x100, 2, 0);
            }
            __builtin_amdgcn_sched_group_barrier(0x008, 3, 0);
            SCHED();
        }
        STAMP(7);
        BAR();
        STAMP(8);
        const LAS float* cs = (const LAS float*)(smem + X_CS);
#pragma unroll
        for (int i = 0; i < 4; ++i) {
            const int ct = 4 * wn + i;
            f32x4 cv[4];
#pragma unroll
            for (int g = 0; g < 4; ++g) cv[g] = *(const LAS f32x4*)(cs + (g & 1) * 128 + 16 * ct + 8 * (g >> 1) + 4 * half);
#pragma unroll
            for (int j = 0; j < 2; ++j) {
                float hv[8];
#pragma unroll
                for (int e = 0; e < 4; ++e) {
                    float g0 = acc[i][j][e] - mean[j] * cv[0][e];
                    float u0 = acc[i][j][4 + e] - mean[j] * cv[1][e];
                    float g1 = acc[i][j][8 + e] - mean[j] * cv[2][e];
                    float u1 = acc[i][j][12 + e] - mean[j] * cv[3][e];
                    hv[e] = fmaxf(g0, 0.f) * u0;
                    hv[4 + e] = fmaxf(g1, 0.f) * u1;
                }
                u32x4 o = {pk2(hv[0], hv[1]), pk2(hv[2], hv[3]), pk2(hv[4], hv[5]), pk2(hv[6], hv[7])};
                const int T = rb * 4 + 2 * wm + j, KS = cb * 8 + ct;
                __builtin_amdgcn_raw_buffer_store_b128(o, rs, WS_HT + (((unsigned)T * 112 + KS) * 64 + lane) * 16, 0, SC1);
            }
        }
        STAMP(9);
    }
    BAR();
    STAMP(10);
    const float Dval = bflo((unsigned)a.Dv[2 * bid + (w >> 2)]);
    const unsigned wdo = WS_WDT + (((unsigned)(ctile * 2) * 112 + 14 * w) * 64 + lane) * 16;
    const unsigned hto = WS_HT + (((unsigned)(rt * 2) * 112 + 14 * w) * 64 + lane) * 16;
    u32x4 wd[14][2];
    const int cw = w >> 2, qh = (w >> 1) & 1, chh = w & 1;
    f32x4 cacc[4];
#pragma unroll
    for (int c = 0; c < 4; ++c) cacc[c] = f32x4{0.f, 0.f, 0.f, 0.f};
    {
        LAS char* cbp = smem + cw * CH_BYTES;
        const int m = lane & 15, g = lane >> 4;
        LAS char* ab0 = cbp + (m & 7) * HC_STRIDE + (16 * qh + g + (m >> 3)) * 16;
        LAS char* gb0 = cbp + 8 * HC_STRIDE + (2032 - 256 * 7 + 128 * qh - 16 * m + 8 * g) * 2;
        auto issue = [&](int qq, int p) {
            const int n = qq * 7 + p;
            if (p < 7) wd[n >> 1][n & 1] = __builtin_amdgcn_raw_buffer_load_b128(rs, wdo + ((n & 1) * 112 + (n >> 1)) * 1024, 0, SC1);
        };
        if (chh) conv_mfma<1>(ab0, gb0, cacc, issue); else conv_mfma<0>(ab0, gb0, cacc, issue);
    }
    f32x4 keep[2];
    {
        f32x4 c0 = cacc[0], c1 = cacc[1], c2 = cacc[2], c3 = cacc[3];
        asm volatile("" : "+v"(c0), "+v"(c1), "+v"(c2), "+v"(c3));
        f32x4 s0, s1;
#pragma unroll
        for (int e = 0; e < 4; ++e) {
            s0[e] = qh ? c0[e] : c2[e]; s1[e] = qh ? c1[e] : c3[e];
            keep[0][e] = qh ? c2[e] : c0[e]; keep[1][e] = qh ? c3[e] : c1[e];
        }
        LAS f32x4* red = (LAS f32x4*)(smem + X_CRED);
        red[(w * 2 + 0) * 64 + lane] = s0;
        red[(w * 2 + 1) * 64 + lane] = s1;
    }
    WAIT_V(0);
    BAR();
    STAMP(11);
    if (tid == 0) (void)__hip_atomic_fetch_add(cnt_ptr(a.ws, C_H + rb), 1u, RLX);
    {
        const LAS f32x4* red = (const LAS f32x4*)(smem + X_CRED);
        const int pw = w ^ 2;
        const int ap = lane & 15, g = lane >> 4;
        const LAS char* xrl = smem + cw * CH_BYTES + 8 * HC_STRIDE;
#pragma unroll
        for (int e2 = 0; e2 < 2; ++e2) {
            const int ci = 2 * qh + e2;
            const int c = chh ? (ci == 0 ? 1 : ci == 1 ? 2 : ci == 2 ? 5 : 6) : (ci == 0 ? 0 : ci == 1 ? 3 : ci == 2 ? 4 : 7);
            f32x4 s = red[(pw * 2 + e2) * 64 + lane] + keep[e2];
            const int s0 = 256 * c + 16 * ap + 4 * g;
            const u32x2 kx = *(const LAS u32x2*)(xrl + (2044 - s0) * 2);
            const float kv0 = bfhi(kx[1]), kv1 = bflo(kx[1]), kv2 = bfhi(kx[0]), kv3 = bflo(kx[0]);
            const float y0 = rbf(s[0]) + rbf(kv0 * Dval), y1 = rbf(s[1]) + rbf(kv1 * Dval);
            const float y2 = rbf(s[2]) + rbf(kv2 * Dval), y3 = rbf(s[3]) + rbf(kv3 * Dval);
            u32x2 o = {pk2(y0, y1), pk2(y2, y3)};
            __builtin_amdgcn_raw_buffer_store_b64(o, rs, WS_YT + ((unsigned)(2 * bid + cw) * S + s0) * 2, 0, SC1);
        }
    }
    if (tid == 0) poll_ge(cnt_ptr(a.ws, C_H + (rt >> 1)), 14u);
    WAIT_V(0);
    BAR();
    STAMP(12);
    if (tid == 0) (void)__hip_atomic_fetch_add(cnt_ptr(a.ws, C_Y + ((2 * bid) >> 6)), 1u, RLX);
    u32x4 hw[HWIN][2];
#pragma unroll
    for (int i = 0; i < HWIN; ++i) {
        hw[i][0] = __builtin_amdgcn_raw_buffer_load_b128(rs, hto + i * 1024, 0, SC1);
        hw[i][1] = __builtin_amdgcn_raw_buffer_load_b128(rs, hto + (112 + i) * 1024, 0, SC1);
    }
    const int tp = tid >> 4, cq = tid & 15;
    const int tok0 = rt * 64 + 2 * tp, d0 = ctile * 64 + 4 * cq;
    u32x2 q0, q1; u32x4 stu;
    SCHED();
    f32x16 acc2[2][2];
#pragma unroll
    for (int i = 0; i < 2; ++i)
#pragma unroll
        for (int j = 0; j < 2; ++j)
#pragma unroll
            for (int e = 0; e < 16; ++e) acc2[i][j][e] = 0.f;
    unsigned yv[4];
#pragma unroll
    for (int i = 0; i < 14; ++i) {
        const u32x4 hf0 = hw[i % HWIN][0], hf1 = hw[i % HWIN][1];
        if (i + HWIN < 14) {
            hw[i % HWIN][0] = __builtin_amdgcn_raw_buffer_load_b128(rs, hto + (i + HWIN) * 1024, 0, SC1);
            hw[i % HWIN][1] = __builtin_amdgcn_raw_buffer_load_b128(rs, hto + (112 + i + HWIN) * 1024, 0, SC1);
        }
        if (i == 9) {
            if (lane == 0) poll_ge(cnt_ptr(a.ws, C_Y + ctile), 32u);
            __builtin_amdgcn_fence(__ATOMIC_ACQUIRE, "wavefront");
#pragma unroll
            for (int j = 0; j < 4; ++j)
                yv[j] = __builtin_amdgcn_raw_buffer_load_b32(rs, WS_YT + (unsigned)(d0 + j) * 4096 + tok0 * 2, 0, SC1);
            q0 = *(const u32x2*)(a.q + (size_t)tok0 * D + d0);
            q1 = *(const u32x2*)(a.q + (size_t)(tok0 + 1) * D + d0);
            stu = __builtin_amdgcn_raw_buffer_load_b128(rs, WS_STATS + (unsigned)tok0 * 8, 0, SC1);
        }
        acc2[0][0] = mfma32u(wd[i][0], hf0, acc2[0][0]);
        acc2[0][1] = mfma32u(wd[i][0], hf1, acc2[0][1]);
        acc2[1][0] = mfma32u(wd[i][1], hf0, acc2[1][0]);
        acc2[1][1] = mfma32u(wd[i][1], hf1, acc2[1][1]);
        SCHED();
    }
    STAMP(13);
    {
        LAS char* rbp = smem + w * (64 * RED_STRIDE);
#pragma unroll
        for (int i = 0; i < 2; ++i)
#pragma unroll
            for (int j = 0; j < 2; ++j)
#pragma unroll
                for (int g = 0; g < 4; ++g) {
                    f32x4 v = {acc2[i][j][4 * g], acc2[i][j][4 * g + 1], acc2[i][j][4 * g + 2], acc2[i][j][4 * g + 3]};
                    *(LAS f32x4*)(rbp + (j * 32 + r) * RED_STRIDE + (i * 32 + 8 * g + 4 * half) * 4) = v;
                }
    }
    BAR();
    {
        f32x4 o[2];
#pragma unroll
        for (int e = 0; e < 2; ++e) {
            const LAS char* rbp = smem + (2 * tp + e) * RED_STRIDE + cq * 16;
            f32x4 s = *(const LAS f32x4*)rbp;
#pragma unroll
            for (int ww = 1; ww < 8; ++ww) s += *(const LAS f32x4*)(rbp + ww * (64 * RED_STRIDE));
            o[e] = s;
        }
#pragma unroll
        for (int e = 0; e < 2; ++e) {
            const float mb = __uint_as_float(stu[2 * e]), rd = __uint_as_float(stu[2 * e + 1]);
            const u32x2 qq = e ? q1 : q0;
            float res[4];
#pragma unroll
            for (int j = 0; j < 4; ++j) {
                const float x = (j & 1) ? bfhi(qq[j >> 1]) : bflo(qq[j >> 1]);
                const float xn = rbf(rbf(x - mb) * rd);
                const float ov = rbf(o[e][j] * rd * rd);
                const float gg = rbf(ov * __builtin_amdgcn_rcpf(1.0f + __expf(-ov)));
                const float y = e ? bfhi(yv[j]) : bflo(yv[j]);
                const float sv = rbf(xn * y);
                res[j] = x + rbf(sv * gg);
            }
            u32x2 ov2 = {pk2(res[0], res[1]), pk2(res[2], res[3])};
            *(u32x2*)(a.out + (size_t)(tok0 + e) * D + d0) = ov2;
        }
    }
    STAMP(15);
    STAMP_FLUSH();
}

extern "C" void kernel_launch(void* const* d_in, const int* in_sizes, int n_in,
                              void* d_out, int out_size, void* d_ws, size_t ws_size,
                              hipStream_t stream) {
    (void)hipFuncSetAttribute((const void*)fused_kernel, hipFuncAttributeMaxDynamicSharedMemorySize, LDS_BYTES);
    Args a;
    a.q = (const u16*)d_in[0]; a.k = (const u16*)d_in[1]; a.v = (const u16*)d_in[2]; a.sk = (const u16*)d_in[3];
    a.ssm = (const u16*)d_in[4]; a.Dv = (const u16*)d_in[5];
    a.wg = (const u16*)d_in[7]; a.wu = (const u16*)d_in[8]; a.wd = (const u16*)d_in[9];
    a.ws = (char*)d_ws; a.out = (u16*)d_out;
    fused_kernel<<<256, 512, LDS_BYTES, stream>>>(a);
}
```

```cpp
#include <hip/hip_runtime.h>
#include <stdint.h>

typedef unsigned short u16;
typedef __attribute__((ext_vector_type(4))) short s16x4;
typedef __attribute__((ext_vector_type(8))) short s16x8;
typedef __attribute__((ext_vector_type(8))) __bf16 bf16x8;
typedef __attribute__((ext_vector_type(2))) __bf16 bf16x2;
typedef __attribute__((ext_vector_type(16))) float f32x16;
typedef __attribute__((ext_vector_type(4))) float f32x4;
typedef __attribute__((ext_vector_type(4))) unsigned u32x4;
typedef __attribute__((ext_vector_type(2))) unsigned u32x2;
#define GAS __attribute__((address_space(1)))
#define LAS __attribute__((address_space(3)))
#define DI __device__ __forceinline__
typedef GAS unsigned gu32;
typedef GAS unsigned long long gu64;
typedef unsigned long long u64;
#define RLX __ATOMIC_RELAXED, __HIP_MEMORY_SCOPE_AGENT

static constexpr int S = 2048, D = 448, F = 1792;
static constexpr int NMAIN = 224, NPREP = 28;
#define STAMP_DECL
#define STAMP(i)
#define STAMP_FLUSH()

static constexpr unsigned SY_M = 0, SY_A = 2048, SY_CNT = 4096;
static constexpr int C_KV = 0, C_ST = 7, C_HS = 21, C_WD = 22, C_H = 29, C_Y = 45, C_CS = 52, C_N = 59;
static constexpr unsigned WS_STATS = 16384;
static constexpr unsigned WS_CS    = WS_STATS + S * 8;
static constexpr unsigned WS_HT    = WS_CS + 16384;
static constexpr unsigned WS_WDT   = WS_HT + S * F * 2;
static constexpr unsigned WS_KVR   = WS_WDT + F * D * 2;
static constexpr unsigned WS_YT    = WS_KVR + S * D * 2;
static constexpr unsigned WS_END   = WS_YT + S * D * 2;
static constexpr int SC1 = 16;
#define TCH0 7
#define LD_AUX 16
static constexpr unsigned SPIN_CAP = 1u << 16;

DI float bflo(unsigned u) { return __uint_as_float(u << 16); }
DI float bfhi(unsigned u) { return __uint_as_float(u & 0xffff0000u); }
DI unsigned pk2(float a, float b) { bf16x2 r = {(__bf16)a, (__bf16)b}; return __builtin_bit_cast(unsigned, r); }
DI float rbf(float a) { return bflo(pk2(a, a)); }
template <int CTRL> DI float dpp_add(float v) {
    return v + __int_as_float(__builtin_amdgcn_update_dpp(0, __float_as_int(v), CTRL, 0xf, 0xf, true));
}
DI float sum8(float v) { v = dpp_add<0xB1>(v); v = dpp_add<0x4E>(v); return dpp_add<0x141>(v); }
DI float xor16_add(float v) { auto r = __builtin_amdgcn_permlane16_swap(__float_as_uint(v), __float_as_uint(v), false, false); return __uint_as_float(r[0]) + __uint_as_float(r[1]); }
DI float xor32_add(float v) { auto r = __builtin_amdgcn_permlane32_swap(__float_as_uint(v), __float_as_uint(v), false, false); return __uint_as_float(r[0]) + __uint_as_float(r[1]); }
DI float sum32(float v) { v = sum8(v); v = dpp_add<0x140>(v); return xor16_add(v); }
DI f32x16 mfma32(s16x8 a, s16x8 b, f32x16 c) {
    return __builtin_amdgcn_mfma_f32_32x32x16_bf16(__builtin_bit_cast(bf16x8, a), __builtin_bit_cast(bf16x8, b), c, 0, 0, 0);
}
DI f32x4 mfma16u(u32x4 a, u32x4 b, f32x4 c) {
    return __builtin_amdgcn_mfma_f32_16x16x32_bf16(__builtin_bit_cast(bf16x8, a), __builtin_bit_cast(bf16x8, b), c, 0, 0, 0);
}
DI f32x16 mfma32u(u32x4 a, u32x4 b, f32x16 c) { return mfma32(__builtin_bit_cast(s16x8, a), __builtin_bit_cast(s16x8, b), c); }
#define WAIT_V(n) asm volatile("s_waitcnt vmcnt(" #n ")" ::: "memory")
#define BAR() do { asm volatile("s_waitcnt lgkmcnt(0)" ::: "memory"); __builtin_amdgcn_s_barrier(); asm volatile("" ::: "memory"); } while (0)
#define SCHED() __builtin_amdgcn_sched_barrier(0)
DI __amdgpu_buffer_rsrc_t mkrs(const void* p, unsigned bytes) { return __builtin_amdgcn_make_buffer_rsrc((void*)p, 0, bytes, 0x00020000); }
DI gu32* cnt_ptr(char* ws, int i) { return (gu32*)(ws + SY_CNT + 128 * i); }
DI void poll_eq(gu32* p, unsigned want) {
    for (unsigned sp = 0; sp < SPIN_CAP; ++sp) { if (__hip_atomic_load(p, RLX) == want) break; __builtin_amdgcn_s_sleep(1); }
}
DI void poll_eq64(gu64* p, u64 want) {
    for (unsigned sp = 0; sp < SPIN_CAP; ++sp) { if (__hip_atomic_load(p, RLX) == want) break; __builtin_amdgcn_s_sleep(1); }
}
DI void poll_ge(gu32* p, unsigned want) {
    for (unsigned sp = 0; sp < SPIN_CAP; ++sp) { if (__hip_atomic_load(p, RLX) >= want) break; __builtin_amdgcn_s_sleep(1); }
}

struct Args {
    const u16* q; const u16* k; const u16* v; const u16* sk; const u16* ssm; const u16* Dv;
    const u16* wg; const u16* wu; const u16* wd;
    char* ws; u16* out;
};

static constexpr int G1_STAGE = 49152;
static constexpr int X_CS = 3 * G1_STAGE;
static constexpr int HC_STRIDE = 4160;
static constexpr int CH_BYTES = 8 * HC_STRIDE + 4608;
static constexpr int X_CRED = 2 * CH_BYTES;
static constexpr int RED_STRIDE = 272;
static constexpr int X_HC = X_CS + 1024;
static constexpr int X_SC = X_HC + 16;
static constexpr int LDS_BYTES = X_SC + 8192;
static constexpr int WWIN = 10;
static constexpr int HWIN = 8;

DI void dma16(const GAS char* sbase, unsigned voff, unsigned ldst) {
    unsigned keep;
    asm volatile("s_mov_b32 %0, m0\n\ts_mov_b32 m0, %3\n\ts_nop 0\n\tglobal_load_lds_dwordx4 %1, %2\n\ts_mov_b32 m0, %0"
                 : "=&s"(keep) : "v"(voff), "s"(sbase), "s"(ldst));
}

DI void service_block(const Args& a, LAS char* smem) {
    const int lane = threadIdx.x & 63, w = threadIdx.x >> 6;
    const __amdgpu_buffer_rsrc_t rs = mkrs(a.ws, WS_END);
    volatile LAS unsigned* stop = (volatile LAS unsigned*)smem;
    if (w == 0) {
        if (lane < C_N) __hip_atomic_store(cnt_ptr(a.ws, lane), 0u, RLX);
        if (lane == 0) *stop = 0u;
        WAIT_V(0);
    }
    BAR();
    if (w == 0) {
        for (unsigned it = 0; it < SPIN_CAP; ++it) {
            const unsigned c = __hip_atomic_load(cnt_ptr(a.ws, lane < C_N ? lane : 0), RLX);
            const bool ok = (lane >= C_CS && lane < C_N) ? (c >= 4u) : (lane < C_ST) ? (c >= 32u) : true;
            if (__all(ok)) break;
            __builtin_amdgcn_s_sleep(8);
        }
        if (lane == 0) *stop = 1u;
    } else if (w < 3) {
        for (int i = 0; i < (w - 1) * 4; ++i) __builtin_amdgcn_s_sleep(2);
        for (unsigned it = 0; it < SPIN_CAP; ++it) {
            const u32x4 m0 = __builtin_amdgcn_raw_buffer_load_b128(rs, SY_M + lane * 32, 0, SC1);
            const u32x4 m1 = __builtin_amdgcn_raw_buffer_load_b128(rs, SY_M + lane * 32 + 16, 0, SC1);
            __builtin_amdgcn_raw_buffer_store_b128(m0, rs, SY_A + lane * 32, 0, SC1);
            __builtin_amdgcn_raw_buffer_store_b128(m1, rs, SY_A + lane * 32 + 16, 0, SC1);
            if (*stop) break;
            __builtin_amdgcn_s_sleep(6);
        }
    }
}

DI u32x2 row_stats(const u32x4 (&xs)[7]) {
    float s0 = 0.f, s1 = 0.f;
#pragma unroll
    for (int i = 0; i < 7; ++i)
#pragma unroll
        for (int j = 0; j < 4; ++j) { s0 += bflo(xs[i][j]); s1 += bfhi(xs[i][j]); }
    float sum = s0 + s1;
    sum = sum8(sum);
    const float m = sum * (1.0f / D);
    float q0 = 0.f, q1 = 0.f;
#pragma unroll
    for (int i = 0; i < 7; ++i)
#pragma unroll
        for (int j = 0; j < 4; ++j) {
            const float d0 = bflo(xs[i][j]) - m, d1 = bfhi(xs[i][j]) - m;
            q0 = fmaf(d0, d0, q0); q1 = fmaf(d1, d1, q1);
        }
    float sq = q0 + q1;
    sq = sum8(sq);
    const float var = sq * (1.0f / D);
    const float mb = rbf(m), vb = rbf(var);
    const float den = rbf(sqrtf(rbf(vb + 1e-5f)));
    return u32x2{__float_as_uint(mb), __float_as_uint(1.0f / den)};
}

#define PSTAMP(i)
DI void prep_block(const Args& a, LAS char* smem) {
    const int tid = threadIdx.x, lane = tid & 63;
    const int w = __builtin_amdgcn_readfirstlane(tid >> 6);
    const int g = (blockIdx.x - NMAIN) * 8 + w;
    const __amdgpu_buffer_rsrc_t rs = mkrs(a.ws, WS_END);
    gu64* mbA = (gu64*)(a.ws + SY_A) + blockIdx.x;
    const u64 chal = __builtin_amdgcn_s_memrealtime();
    if (tid == 0) __hip_atomic_store((gu64*)(a.ws + SY_M) + blockIdx.x, chal, RLX);
    const int hb = blockIdx.x - NMAIN;
    const u16* wsrc = (hb < 14 ? a.wg : a.wu) + (hb < 14 ? hb : hb - 14) * 128 + (tid & 15) * 8;
    u32x4 wr[14];
#pragma unroll
    for (int i = 0; i < 14; ++i) wr[i] = *(const u32x4*)(wsrc + (size_t)((tid >> 4) * 14 + i) * F);
    u32x4 xs[7];
    if (g < 32) {
        const int srow = (224 + g) * 8 + (lane >> 3);
#pragma unroll
        for (int i = 0; i < 7; ++i) xs[i] = *(const u32x4*)(a.q + (size_t)srow * D + ((lane & 7) + 8 * i) * 8);
    }
    SCHED();
    PSTAMP(1);
    float cs[8];
#pragma unroll
    for (int j = 0; j < 8; ++j) cs[j] = 0.f;
#pragma unroll
    for (int i = 0; i < 14; ++i)
#pragma unroll
        for (int e = 0; e < 4; ++e) { cs[2 * e] += bflo(wr[i][e]); cs[2 * e + 1] += bfhi(wr[i][e]); }
#pragma unroll
    for (int j = 0; j < 8; ++j) cs[j] = xor32_add(xor16_add(cs[j]));
    LAS f32x4* red = (LAS f32x4*)smem;
    if (lane < 16) { red[(w * 16 + lane) * 2] = f32x4{cs[0], cs[1], cs[2], cs[3]}; red[(w * 16 + lane) * 2 + 1] = f32x4{cs[4], cs[5], cs[6], cs[7]}; }
    if (g < 32) {
        const u32x2 o = row_stats(xs);
        if ((lane & 7) == 0) __builtin_amdgcn_raw_buffer_store_b64(o, rs, WS_STATS + (unsigned)((224 + g) * 8 + (lane >> 3)) * 8, 0, SC1);
    }
    BAR();
    if (tid < 32) {
        f32x4 s = red[tid];
#pragma unroll
        for (int ww = 1; ww < 8; ++ww) s += red[ww * 32 + tid];
        __builtin_amdgcn_raw_buffer_store_b128(__builtin_bit_cast(u32x4, s), rs, WS_CS + (unsigned)((hb < 14 ? 0 : 1792) + (hb < 14 ? hb : hb - 14) * 128 + tid * 4) * 4, 0, SC1);
    }
    PSTAMP(4);
    WAIT_V(0);
    PSTAMP(5);
    BAR();
    PSTAMP(6);
    if (w == 0) {
        if (lane == 0) poll_eq64(mbA, chal);
        const int cbh = hb < 14 ? hb : hb - 14;
        if (lane == 0 || (lane == 1 && hb < 4)) (void)__hip_atomic_fetch_add(cnt_ptr(a.ws, lane == 0 ? C_CS + (cbh >> 1) : C_HS), 1u, RLX);
    }
}

__global__ __launch_bounds__(512, 2) void fused_kernel(Args a) {
    extern __shared__ __attribute__((aligned(16))) char smem_[];
    LAS char* smem = (LAS char*)smem_;
    const int bid = blockIdx.x;
    STAMP_DECL
    STAMP(0);
    if (bid >= NMAIN) {
        if (bid == NMAIN + NPREP) service_block(a, smem); else prep_block(a, smem);
        STAMP(15); STAMP_FLUSH();
        return;
    }
    const int tid = threadIdx.x, lane = tid & 63;
    const int w = __builtin_amdgcn_readfirstlane(tid >> 6);
    const int xcd = bid & 7, idx = bid >> 3;
    const __amdgpu_buffer_rsrc_t rs = mkrs(a.ws, WS_END);
    const int rb = (xcd >> 1) * 4 + (idx & 3);
    const int cb = (xcd & 1) * 7 + (idx >> 2);
    const int rt = xcd * 4 + (idx & 3);
    const int ctile = idx >> 2;
    const int r = lane & 31, half = lane >> 5;
    const int rot = (2 * ctile + 3 * (idx & 3) + (idx & 2) / 2) % 14;
    const unsigned wdo = WS_WDT + (((unsigned)(ctile * 2) * 112 + 14 * w) * 64 + lane) * 16;
    const unsigned hto = WS_HT + (((unsigned)(rt * 2) * 112 + 14 * w) * 64 + lane) * 16;
    auto kso = [&](int i) { const int k = i + rot; return (unsigned)(k >= 14 ? k - 14 : k) * 1024u; };
    u32x4 wd[WWIN][2];
    auto wdload = [&](int n) {
        wd[(n >> 1) % WWIN][n & 1] = __builtin_amdgcn_raw_buffer_load_b128(rs, wdo + (n & 1) * 112 * 1024, kso(n >> 1), LD_AUX);
    };
    if (w >= 4) {
        const int lw = w - 4, lt = tid - 256;
        gu64* mbA = (gu64*)(a.ws + SY_A) + bid;
        const u64 chal = __builtin_amdgcn_s_memrealtime();
        if (tid == 256) __hip_atomic_store((gu64*)(a.ws + SY_M) + bid, chal, RLX);
        const GAS char* qb = (const GAS char*)a.q + (size_t)rb * 128 * D * 2;
        const GAS char* wgb = (const GAS char*)a.wg + (size_t)cb * 256;
        const GAS char* wub = (const GAS char*)a.wu + (size_t)cb * 256;
        unsigned xo[2], wo[2];
#pragma unroll
        for (int e = 0; e < 2; ++e) {
            const int row = 8 * e + (lane >> 3);
            xo[e] = row * (D * 2) + (((lane & 7) ^ ((row >> 1) & 7)) * 16);
            const int pc = (lane & 15) ^ (((lane >> 4) << 2) | (e << 1));
            wo[e] = (lane >> 4) * (F * 2) + pc * 16;
        }
        const unsigned lds0 = (unsigned)(uintptr_t)smem;
        auto piece = [&](int c, int m) {
            const unsigned sb = lds0 + (c % 3) * G1_STAGE;
            const int n = m / 3, i = 4 * lw + n;
            if (m % 3 == 0) dma16(wgb + (size_t)(c * 64 + 4 * i) * (F * 2), wo[0], sb + 16384 + i * 1024);
            else if (m % 3 == 1) dma16(wub + (size_t)(c * 64 + 4 * i) * (F * 2), wo[1], sb + 32768 + i * 1024);
            else dma16(qb + c * 128 + (size_t)(i >> 1) * 16 * (D * 2), xo[n & 1], sb + i * 1024);
        };
        auto stage = [&](int c) {
#pragma unroll
            for (int m = 0; m < 12; ++m) piece(c, m);
        };
        STAMP(24);
        stage(0);
        STAMP(25);
        stage(1);
        STAMP(26);
        u32x4 xr[2];
        WAIT_V(12);
        STAMP(28);
        BAR();
        STAMP(1);
        u32x4 h0[2], h1[2];
#pragma unroll
        for (int c2 = 0; c2 < 2; ++c2) {
            const u16* hs = a.ssm + (size_t)(2 * bid + c2) * S;
            h0[c2] = *(const u32x4*)(hs + (lt > 0 ? lt - 1 : 0) * 8);
            h1[c2] = *(const u32x4*)(hs + lt * 8);
        }
        if (tid == 256) { *(LAS unsigned*)(smem + X_HC) = 0u; *(LAS unsigned*)(smem + X_HC + 4) = 0u; }
        u32x2 ans = {0u, 0u};
        u32x4 wt[2];
#pragma unroll
        for (int c = 0; c < 6; ++c) {
            if (c == 0 && lw == 0) asm volatile("global_load_dwordx2 %0, %1, %2 sc1" : "=v"(ans) : "v"(0u), "s"(mbA));
            unsigned cnt = 0u;
            const int ci = (lane == 1) ? (rb < 14 ? C_ST + rb : C_HS) : (lane == 2) ? C_WD + ctile : (lane == 3) ? C_CS + (cb >> 1) : C_KV + (bid >> 5);
            const unsigned want = (lane == 1) ? (rb < 14 ? 16u : 4u) : (lane == 3) ? 4u : 32u;
            if (c == 5 && lw == 0) cnt = __hip_atomic_load(cnt_ptr(a.ws, ci), RLX);
            if (c == 0) {
#pragma unroll
                for (int e = 0; e < 2; ++e) {
                    const int t = 7 * bid + (lw < 3 ? 2 * lw + e : 6), dt = t / 112, ks = t % 112;
                    const GAS char* p = (const GAS char*)a.wd + ((size_t)(16 * ks) * D + 32 * dt) * 2;
                    asm volatile("global_load_dwordx4 %0, %1, %2" : "=v"(wt[e]) : "v"((unsigned)((lane >> 2) * (D * 2) + (lane & 3) * 16)), "s"(p));
                }
            }
            if (c == 1) {
                LAS char* sc = smem + X_SC + lw * 2048;
                const int gi = lane >> 4, hf = gi >> 1, blk = gi & 1, q4 = (lane >> 2) & 3, p4 = lane & 3;
                const int ra = (4 * hf + q4) * 64 + (16 * blk + 4 * p4) * 2;
                *(LAS u32x4*)(sc + lane * 16) = wt[0];
                *(LAS u32x4*)(sc + 1024 + lane * 16) = wt[1];
                asm volatile("s_waitcnt lgkmcnt(0)" ::: "memory");
#pragma unroll
                for (int e = 0; e < 2; ++e) {
                    s16x4 lo = __builtin_amdgcn_ds_read_tr16_b64_v4i16((LAS s16x4*)(sc + e * 1024 + ra));
                    s16x4 hi = __builtin_amdgcn_ds_read_tr16_b64_v4i16((LAS s16x4*)(sc + e * 1024 + ra + 512));
                    s16x8 o = __builtin_shufflevector(lo, hi, 0, 1, 2, 3, 4, 5, 6, 7);
                    const int t = 7 * bid + (lw < 3 ? 2 * lw + e : 6);
                    __builtin_amdgcn_raw_buffer_store_b128(__builtin_bit_cast(u32x4, o), rs, WS_WDT + (unsigned)t * 1024 + lane * 16, 0, SC1);
                }
                SCHED();
            }
            if (c == 2 && tid == 256) (void)__hip_atomic_fetch_add(cnt_ptr(a.ws, C_WD + (bid >> 5)), 1u, RLX);
            if (c == 2) STAMP(23);
            if (c == 1 && lw == 0) {
                if ((unsigned)__builtin_amdgcn_readfirstlane(ans[0]) != (unsigned)chal || (unsigned)__builtin_amdgcn_readfirstlane(ans[1]) != (unsigned)(chal >> 32)) { if (lane == 0) poll_eq64(mbA, chal); }
                if (lane < 2) (void)__hip_atomic_fetch_add(cnt_ptr(a.ws, lane == 0 ? C_KV + bid % 7 : C_ST + (bid >> 4)), 1u, RLX);
                STAMP(22);
            }
            if (c + 2 <= 6) stage(c + 2);
            STAMP(16 + c);
            if (c == 5) {
                if (lw == 0) {
                    for (unsigned sp = 0; sp < SPIN_CAP; ++sp) {
                        if (__all(cnt >= want)) break;
                        __builtin_amdgcn_s_sleep(1);
                        cnt = __hip_atomic_load(cnt_ptr(a.ws, ci), RLX);

                    }
                }
                STAMP(20);
                asm volatile("" ::: "memory");
                WAIT_V(0);
                STAMP(31);
                asm volatile("" ::: "memory");
            } else if (c == 0) asm volatile("s_waitcnt vmcnt(12)" : "+v"(ans), "+v"(wt[0]), "+v"(wt[1]) :: "memory");
            else WAIT_V(12);
            BAR();
            STAMP(2 + c);
        }
        const u32x4 cv = __builtin_amdgcn_raw_buffer_load_b128(rs, WS_CS + (unsigned)((lane >> 5) * 1792 + cb * 128 + (lane & 31) * 4) * 4, 0, SC1);
#pragma unroll
        for (int c2 = 0; c2 < 2; ++c2)
            xr[c2] = __builtin_amdgcn_raw_buffer_load_b128(rs, WS_KVR + ((unsigned)(2 * bid + c2) * S + lt * 8) * 2, 0, SC1);
        if (lw == 0) *(LAS u32x4*)(smem + X_CS + lane * 16) = cv;
        BAR();
        STAMP(8);
#pragma unroll
        for (int c2 = 0; c2 < 2; ++c2) {
            LAS char* cbp = smem + c2 * CH_BYTES;
            u32x4 hh0 = h0[c2];
            if (lt == 0) hh0 = u32x4{0, 0, 0, 0};
            const unsigned d[8] = {hh0[0], hh0[1], hh0[2], hh0[3], h1[c2][0], h1[c2][1], h1[c2][2], h1[c2][3]};
            unsigned al[7];
#pragma unroll
            for (int i = 0; i < 7; ++i) al[i] = __builtin_amdgcn_alignbit(d[i + 1], d[i], 16);
            const int t = lt + 1;
#pragma unroll
            for (int sg = 0; sg < 8; ++sg) {
                const int sh = sg + 1;
                u32x4 o;
#pragma unroll
                for (int e = 0; e < 4; ++e) o[e] = (sh & 1) ? al[(sh >> 1) + e] : d[(sh >> 1) + e];
                *(LAS u32x4*)(cbp + sg * HC_STRIDE + t * 16) = o;
                if ((sg & 1) == 0) { wdload(c2 * 4 + (sg >> 1)); SCHED(); }
            }
            if (lt < 8) *(LAS u32x4*)(cbp + lt * HC_STRIDE) = u32x4{0, 0, 0, 0};
            *(LAS u32x4*)(cbp + 8 * HC_STRIDE + lt * 16) = xr[c2];
            if (lt < 32) *(LAS u32x4*)(cbp + 8 * HC_STRIDE + 4096 + lt * 16) = u32x4{0, 0, 0, 0};
        }
        STAMP(9);
    } else {
        const int s0 = (bid / 7) * 64 + (2 * w + half) * 8;
        const int dch = (bid % 7) * 64 + 2 * r;
        unsigned kr[11], vr[8];
#pragma unroll
        for (int i = 0; i < 11; ++i) {
            const int s = s0 - 3 + i;
            asm volatile("global_load_dword %0, %1, %2" : "=v"(kr[i]) : "v"((unsigned)((s < 0 ? 0 : s) * D + dch) * 2u), "s"((const GAS char*)a.k));
        }
#pragma unroll
        for (int i = 0; i < 8; ++i)
            asm volatile("global_load_dword %0, %1, %2" : "=v"(vr[i]) : "v"((unsigned)((s0 + i) * D + dch) * 2u), "s"((const GAS char*)a.v));
        u32x4 sk;
        asm volatile("global_load_dwordx4 %0, %1, %2" : "=v"(sk) : "v"((unsigned)dch * 8u), "s"((const GAS char*)a.sk));
        const int srow = 8 * bid + 2 * w + half, l5 = r < 28 ? r : 27;
        u32x4 sx[2];
        asm volatile("global_load_dwordx4 %0, %1, %2" : "=v"(sx[0]) : "v"((unsigned)(srow * D + l5 * 16) * 2u), "s"((const GAS char*)a.q));
        asm volatile("global_load_dwordx4 %0, %1, %2 offset:16" : "=v"(sx[1]) : "v"((unsigned)(srow * D + l5 * 16) * 2u), "s"((const GAS char*)a.q));
        float ko[8], smean = 0.f;
        unsigned kpk[4];
        auto kv_part = [&](int c, int hp) {
            const float w0 = bflo(sk[2 * c]), w1 = bfhi(sk[2 * c]), w2 = bflo(sk[2 * c + 1]), w3 = bfhi(sk[2 * c + 1]);
            float kf[7];
#pragma unroll
            for (int i = 0; i < 7; ++i) {
                unsigned kk = kr[4 * hp + i];
                if (hp == 0 && i < 3) kk = (s0 == 0) ? 0u : kk;
                kf[i] = c ? bfhi(kk) : bflo(kk);
            }
#pragma unroll
            for (int i = 0; i < 4; ++i) {
                float acc = kf[i] * w3;
                acc = fmaf(kf[i + 1], w2, acc);
                acc = fmaf(kf[i + 2], w1, acc);
                acc = fmaf(kf[i + 3], w0, acc);
                ko[4 * hp + i] = acc * (c ? bfhi(vr[4 * hp + i]) : bflo(vr[4 * hp + i]));
            }
            kpk[3 - 2 * hp] = pk2(ko[4 * hp + 1], ko[4 * hp + 0]);
            kpk[2 - 2 * hp] = pk2(ko[4 * hp + 3], ko[4 * hp + 2]);
            if (hp == 1)
                __builtin_amdgcn_raw_buffer_store_b128(u32x4{kpk[0], kpk[1], kpk[2], kpk[3]}, rs, WS_KVR + ((unsigned)(dch + c) * S + (2040 - s0)) * 2, 0, SC1);
        };
        auto st_part = [&](int p) {
            if (p == 0) {
                float s = 0.f;
#pragma unroll
                for (int i = 0; i < 2; ++i)
#pragma unroll
                    for (int j = 0; j < 4; ++j) s += bflo(sx[i][j]) + bfhi(sx[i][j]);
                s = r < 28 ? s : 0.f;
                s = sum32(s);
                smean = s * (1.0f / D);
            } else {
                float s = 0.f;
#pragma unroll
                for (int i = 0; i < 2; ++i)
#pragma unroll
                    for (int j = 0; j < 4; ++j) {
                        const float d0 = bflo(sx[i][j]) - smean, d1 = bfhi(sx[i][j]) - smean;
                        s = fmaf(d0, d0, s); s = fmaf(d1, d1, s);
                    }
                s = r < 28 ? s : 0.f;
                s = sum32(s);
                const float var = s * (1.0f / D);
                const float mb = rbf(smean), vb = rbf(var);
                const float den = rbf(sqrtf(rbf(vb + 1e-5f)));
                if (r == 0) __builtin_amdgcn_raw_buffer_store_b64(u32x2{__float_as_uint(mb), __float_as_uint(1.0f / den)}, rs, WS_STATS + (unsigned)srow * 8, 0, SC1);
            }
        };
        const int wm = w >> 1, wn = w & 1;
        unsigned xa[4], wa[4];
        {
            const int gi = lane >> 4, blk = gi & 1, q4 = (lane >> 2) & 3, p4 = lane & 3, gu = p4 >> 1;
#pragma unroll
            for (int t = 0; t < 4; ++t) xa[t] = (unsigned)(wm * 64 + r) * 128 + (((2 * t + half) ^ ((r >> 1) & 7)) * 16);
#pragma unroll
            for (int i = 0; i < 4; ++i) {
                const int pc = 2 * (4 * wn + i) + blk;
                wa[i] = 16384 + gu * 16384 + (8 * half + q4) * 256 + ((pc ^ ((q4 << 2) | (gu << 1))) * 16) + (p4 & 1) * 8;
            }
        }
        f32x16 acc[4][2];
#pragma unroll
        for (int i = 0; i < 4; ++i)
#pragma unroll
            for (int j = 0; j < 2; ++j)
#pragma unroll
                for (int e = 0; e < 16; ++e) acc[i][j][e] = 0.f;
        s16x8 xf[2][2], wf[2][4];
        auto rdfrag = [&](int ks) {
            const int c = ks >> 2, t = ks & 3, b = ks & 1;
            LAS char* sb = smem + (c % 3) * G1_STAGE;
#pragma unroll
            for (int j = 0; j < 2; ++j) xf[b][j] = *(const LAS s16x8*)(sb + xa[t] + j * 4096);
#pragma unroll
            for (int i = 0; i < 4; ++i) {
                s16x4 lo = __builtin_amdgcn_ds_read_tr16_b64_v4i16((LAS s16x4*)(sb + wa[i] + t * 4096));
                s16x4 hi = __builtin_amdgcn_ds_read_tr16_b64_v4i16((LAS s16x4*)(sb + wa[i] + t * 4096 + 1024));
                wf[b][i] = __builtin_shufflevector(lo, hi, 0, 1, 2, 3, 4, 5, 6, 7);
            }
        };
        float mean[2];
        asm volatile("s_waitcnt vmcnt(0)" : "+v"(kr[0]), "+v"(kr[1]), "+v"(kr[2]), "+v"(kr[3]), "+v"(kr[4]), "+v"(kr[5]), "+v"(kr[6]), "+v"(kr[7]), "+v"(kr[8]), "+v"(kr[9]), "+v"(kr[10]),
                     "+v"(vr[0]), "+v"(vr[1]), "+v"(vr[2]), "+v"(vr[3]), "+v"(vr[4]), "+v"(vr[5]), "+v"(vr[6]), "+v"(vr[7]), "+v"(sk), "+v"(sx[0]), "+v"(sx[1]));
        STAMP(24);
        kv_part(0, 0); kv_part(0, 1); kv_part(1, 0); kv_part(1, 1);
        st_part(0); st_part(1);
        STAMP(25);
        BAR();
        STAMP(1);
        unsigned tch[2] = {0u, 0u};
        rdfrag(0);
#pragma unroll
        for (int ks = 0; ks < 28; ++ks) {
            const int c = ks >> 2, t = ks & 3, b = ks & 1;
            if (t == 3 && c < 6) {
                if (c == 0) asm volatile("s_waitcnt vmcnt(0)" : "+v"(tch[0]), "+v"(tch[1]) :: "memory");
                if (c == 5) STAMP(6);
                BAR();
                if (c == 5) {
#pragma unroll
                    for (int j = 0; j < 2; ++j)
                        mean[j] = __uint_as_float(__builtin_amdgcn_raw_buffer_load_b32(rs, WS_STATS + (unsigned)(rb * 128 + (2 * wm + j) * 32 + r) * 8, 0, SC1));
                }
            }
            if (ks < 27) rdfrag(ks + 1);
#pragma unroll
            for (int i = 0; i < 4; ++i) {
                acc[i][0] = mfma32(wf[b][i], xf[b][0], acc[i][0]);
                acc[i][1] = mfma32(wf[b][i], xf[b][1], acc[i][1]);
            }
#pragma unroll
            for (int i = 0; i < 8; ++i) {
                __builtin_amdgcn_sched_group_barrier(0x008, 1, 0);
                if (i < 5) __builtin_amdgcn_sched_group_barrier(0x100, 2, 0);
            }
            SCHED();
        }
        STAMP(7);
        BAR();
        STAMP(8);
#pragma unroll
        for (int n = 0; n < 8; ++n) wdload(n);
        SCHED();
        const LAS float* cs = (const LAS float*)(smem + X_CS);
#pragma unroll
        for (int i = 0; i < 4; ++i) {
            const int ct = 4 * wn + i;
            f32x4 cv[4];
#pragma unroll
            for (int g = 0; g < 4; ++g) cv[g] = *(const LAS f32x4*)(cs + (g & 1) * 128 + 16 * ct + 8 * (g >> 1) + 4 * half);
#pragma unroll
            for (int j = 0; j < 2; ++j) {
                float hv[8];
#pragma unroll
                for (int e = 0; e < 4; ++e) {
                    float g0 = acc[i][j][e] - mean[j] * cv[0][e];
                    float u0 = acc[i][j][4 + e] - mean[j] * cv[1][e];
                    float g1 = acc[i][j][8 + e] - mean[j] * cv[2][e];
                    float u1 = acc[i][j][12 + e] - mean[j] * cv[3][e];
                    hv[e] = fmaxf(g0, 0.f) * u0;
                    hv[4 + e] = fmaxf(g1, 0.f) * u1;
                }
                u32x4 o = {pk2(hv[0], hv[1]), pk2(hv[2], hv[3]), pk2(hv[4], hv[5]), pk2(hv[6], hv[7])};
                const int T = rb * 4 + 2 * wm + j, KS = cb * 8 + ct;
                __builtin_amdgcn_raw_buffer_store_b128(o, rs, WS_HT + (((unsigned)T * 112 + KS) * 64 + lane) * 16, 0, SC1);
            }
        }
        STAMP(9);
    }
    const float Dval = bflo((unsigned)a.Dv[2 * bid + (w >> 2)]);
    BAR();
    STAMP(10);
    const int cw = w >> 2, qp = w & 3;
    f32x4 cacc[8];
#pragma unroll
    for (int c = 0; c < 8; ++c) cacc[c] = f32x4{0.f, 0.f, 0.f, 0.f};
    {
        LAS char* cbp = smem + cw * CH_BYTES;
        const int m = lane & 15, g = lane >> 4;
        const LAS char* ab0 = cbp + (m & 7) * HC_STRIDE + (8 * qp + g + (m >> 3)) * 16;
        const LAS char* gb0 = cbp + 8 * HC_STRIDE + (2032 - 256 * 7 + 64 * qp - 16 * m + 8 * g) * 2;
        u32x4 A[8], G[8], A2[8], G2[8];
#pragma unroll
        for (int p = 0; p < 8; ++p) {
            G[p] = *(const LAS u32x4*)(gb0 + (7 - p) * 512);
            A[p] = *(const LAS u32x4*)(ab0 + p * 512);
        }
        SCHED();
#pragma unroll
        for (int p = 0; p < 8; ++p) {
#pragma unroll
            for (int c = p; c < 8; ++c) cacc[c] = mfma16u(A[p], G[c - p], cacc[c]);
            A2[p] = *(const LAS u32x4*)(ab0 + 64 + p * 512);
            G2[7 - p] = *(const LAS u32x4*)(gb0 + 64 + p * 512);
            if (p < 6) { wdload(8 + 2 * p); wdload(9 + 2 * p); }
            if (p == 3 && w < 4) {
                WAIT_V(8);
                unsigned old = 0u;
                if (lane == 0) old = __hip_atomic_fetch_add((LAS unsigned*)(smem + X_HC), 1u, __ATOMIC_RELAXED, __HIP_MEMORY_SCOPE_WORKGROUP);
                if (lane == 0 && old == 3u) (void)__hip_atomic_fetch_add(cnt_ptr(a.ws, C_H + rb), 1u, RLX);
            }
            SCHED();
        }
#pragma unroll
        for (int mm = 7; mm >= 0; --mm) {
#pragma unroll
            for (int p = 0; p + mm < 8; ++p) cacc[p + mm] = mfma16u(A2[p], G2[mm], cacc[p + mm]);
        }
    }
    f32x4 keep[2];
    {
        LAS f32x4* red = (LAS f32x4*)(smem + X_CRED) + w * 512;
#pragma unroll
        for (int j = 0; j < 4; ++j) {
            if (qp != j) { red[(2 * j) * 64 + lane] = cacc[2 * j]; red[(2 * j + 1) * 64 + lane] = cacc[2 * j + 1]; }
            else { keep[0] = cacc[2 * j]; keep[1] = cacc[2 * j + 1]; }
        }
    }
    BAR();
    STAMP(11);
    {
        const LAS f32x4* red = (const LAS f32x4*)(smem + X_CRED) + cw * 2048;
        const int ap = lane & 15, g = lane >> 4;
        const LAS char* xrl = smem + cw * CH_BYTES + 8 * HC_STRIDE;
#pragma unroll
        for (int e2 = 0; e2 < 2; ++e2) {
            const int c = 2 * qp + e2;
            f32x4 s = keep[e2];
#pragma unroll
            for (int j = 1; j < 4; ++j) s += red[((qp + j) & 3) * 512 + c * 64 + lane];
            const int s0 = 256 * c + 16 * ap + 4 * g;
            const u32x2 kx = *(const LAS u32x2*)(xrl + (2044 - s0) * 2);
            const float kv0 = bfhi(kx[1]), kv1 = bflo(kx[1]), kv2 = bfhi(kx[0]), kv3 = bflo(kx[0]);
            const float y0 = rbf(s[0]) + rbf(kv0 * Dval), y1 = rbf(s[1]) + rbf(kv1 * Dval);
            const float y2 = rbf(s[2]) + rbf(kv2 * Dval), y3 = rbf(s[3]) + rbf(kv3 * Dval);
            u32x2 o = {pk2(y0, y1), pk2(y2, y3)};
            __builtin_amdgcn_raw_buffer_store_b64(o, rs, WS_YT + ((unsigned)(2 * bid + cw) * S + s0) * 2, 0, SC1);
        }
    }
    if (lane == 0) poll_ge(cnt_ptr(a.ws, C_H + (rt >> 1)), 14u);
    WAIT_V(0);
    {
        unsigned old = 0u;
        if (lane == 0) old = __hip_atomic_fetch_add((LAS unsigned*)(smem + X_HC + 4), 1u, __ATOMIC_RELAXED, __HIP_MEMORY_SCOPE_WORKGROUP);
        if (lane == 0 && old == 7u) (void)__hip_atomic_fetch_add(cnt_ptr(a.ws, C_Y + ((2 * bid) >> 6)), 1u, RLX);
    }
    STAMP(12);
    u32x4 hw[HWIN][2];
    auto hload = [&](int i) {
        hw[i % HWIN][0] = __builtin_amdgcn_raw_buffer_load_b128(rs, hto, kso(i), LD_AUX);
        hw[i % HWIN][1] = __builtin_amdgcn_raw_buffer_load_b128(rs, hto + 112 * 1024, kso(i), LD_AUX);
    };
#pragma unroll
    for (int i = 0; i < HWIN; ++i) hload(i);
    const int tp = tid >> 4, cq = tid & 15;
    const int tok0 = rt * 64 + 2 * tp, d0 = ctile * 64 + 4 * cq;
    u32x2 q0, q1; u32x4 stu;
    SCHED();
    BAR();
    f32x16 acc2[2][2];
#pragma unroll
    for (int i = 0; i < 2; ++i)
#pragma unroll
        for (int j = 0; j < 2; ++j)
#pragma unroll
            for (int e = 0; e < 16; ++e) acc2[i][j][e] = 0.f;
    unsigned yv[4];
#pragma unroll
    for (int i = 0; i < 14; ++i) {
        const u32x4 hf0 = hw[i % HWIN][0], hf1 = hw[i % HWIN][1];
        if (i + HWIN < 14) hload(i + HWIN);
        if (i == 12) {
            if (lane == 0) poll_ge(cnt_ptr(a.ws, C_Y + ctile), 32u);
            __builtin_amdgcn_fence(__ATOMIC_ACQUIRE, "wavefront");
#pragma unroll
            for (int j = 0; j < 4; ++j)
                yv[j] = __builtin_amdgcn_raw_buffer_load_b32(rs, WS_YT + (unsigned)(d0 + j) * 4096 + tok0 * 2, 0, SC1);
            q0 = *(const u32x2*)(a.q + (size_t)tok0 * D + d0);
            q1 = *(const u32x2*)(a.q + (size_t)(tok0 + 1) * D + d0);
            stu = __builtin_amdgcn_raw_buffer_load_b128(rs, WS_STATS + (unsigned)tok0 * 8, 0, SC1);
        }
        const u32x4 wf0 = wd[i % WWIN][0], wf1 = wd[i % WWIN][1];
        if (i + WWIN < 14) { wdload(2 * (i + WWIN)); wdload(2 * (i + WWIN) + 1); }
        acc2[0][0] = mfma32u(wf0, hf0, acc2[0][0]);
        acc2[0][1] = mfma32u(wf0, hf1, acc2[0][1]);
        acc2[1][0] = mfma32u(wf1, hf0, acc2[1][0]);
        acc2[1][1] = mfma32u(wf1, hf1, acc2[1][1]);
        SCHED();
    }
    STAMP(13);
    {
        LAS char* rbp = smem + w * (64 * RED_STRIDE);
#pragma unroll
        for (int i = 0; i < 2; ++i)
#pragma unroll
            for (int j = 0; j < 2; ++j)
#pragma unroll
                for (int g = 0; g < 4; ++g) {
                    f32x4 v = {acc2[i][j][4 * g], acc2[i][j][4 * g + 1], acc2[i][j][4 * g + 2], acc2[i][j][4 * g + 3]};
                    *(LAS f32x4*)(rbp + (j * 32 + r) * RED_STRIDE + (i * 32 + 8 * g + 4 * half) * 4) = v;
                }
    }
    BAR();
    {
        f32x4 o[2];
#pragma unroll
        for (int e = 0; e < 2; ++e) {
            const LAS char* rbp = smem + (2 * tp + e) * RED_STRIDE + cq * 16;
            f32x4 s = *(const LAS f32x4*)rbp;
#pragma unroll
            for (int ww = 1; ww < 8; ++ww) s += *(const LAS f32x4*)(rbp + ww * (64 * RED_STRIDE));
            o[e] = s;
        }
#pragma unroll
        for (int e = 0; e < 2; ++e) {
            const float mb = __uint_as_float(stu[2 * e]), rd = __uint_as_float(stu[2 * e + 1]);
            const u32x2 qq = e ? q1 : q0;
            float res[4];
#pragma unroll
            for (int j = 0; j < 4; ++j) {
                const float x = (j & 1) ? bfhi(qq[j >> 1]) : bflo(qq[j >> 1]);
                const float xn = rbf(rbf(x - mb) * rd);
                const float ov = rbf(o[e][j] * rd * rd);
                const float gg = rbf(ov * __builtin_amdgcn_rcpf(1.0f + __expf(-ov)));
                const float y = e ? bfhi(yv[j]) : bflo(yv[j]);
                const float sv = rbf(xn * y);
                res[j] = x + rbf(sv * gg);
            }
            u32x2 ov2 = {pk2(res[0], res[1]), pk2(res[2], res[3])};
            __builtin_amdgcn_raw_buffer_store_b64(ov2, mkrs(a.out, S * D * 2), (unsigned)((tok0 + e) * D + d0) * 2u, 0, 16);
        }
    }
    STAMP(15);
    STAMP_FLUSH();
}

extern "C" void kernel_launch(void* const* d_in, const int* in_sizes, int n_in,
                              void* d_out, int out_size, void* d_ws, size_t ws_size,
                              hipStream_t stream) {
    (void)hipFuncSetAttribute((const void*)fused_kernel, hipFuncAttributeMaxDynamicSharedMemorySize, LDS_BYTES);
    Args a;
    a.q = (const u16*)d_in[0]; a.k = (const u16*)d_in[1]; a.v = (const u16*)d_in[2]; a.sk = (const u16*)d_in[3];
    a.ssm = (const u16*)d_in[4]; a.Dv = (const u16*)d_in[5];
    a.wg = (const u16*)d_in[7]; a.wu = (const u16*)d_in[8]; a.wd = (const u16*)d_in[9];
    a.ws = (char*)d_ws; a.out = (u16*)d_out;
    fused_kernel<<<NMAIN + NPREP + 1, 512, LDS_BYTES, stream>>>(a);
}
```

```cpp
#include <hip/hip_runtime.h>
#include <stdint.h>

typedef unsigned short u16;
typedef __attribute__((ext_vector_type(4))) short s16x4;
typedef __attribute__((ext_vector_type(8))) short s16x8;
typedef __attribute__((ext_vector_type(8))) __bf16 bf16x8;
typedef __attribute__((ext_vector_type(2))) __bf16 bf16x2;
typedef __attribute__((ext_vector_type(16))) float f32x16;
typedef __attribute__((ext_vector_type(4))) float f32x4;
typedef __attribute__((ext_vector_type(4))) unsigned u32x4;
typedef __attribute__((ext_vector_type(2))) unsigned u32x2;
#define GAS __attribute__((address_space(1)))
#define LAS __attribute__((address_space(3)))
#define DI __device__ __forceinline__
typedef GAS unsigned gu32;
typedef GAS unsigned long long gu64;
typedef unsigned long long u64;
#define RLX __ATOMIC_RELAXED, __HIP_MEMORY_SCOPE_AGENT

static constexpr int S = 2048, D = 448, F = 1792;
static constexpr int NMAIN = 224, NPREP = 28;
#define STAMP_DECL
#define STAMP(i)
#define STAMP_FLUSH()

static constexpr unsigned SY_M = 0, SY_A = 2048, SY_CNT = 4096;
static constexpr int C_KV = 0, C_ST = 7, C_HS = 21, C_WD = 22, C_H = 29, C_Y = 45, C_CS = 52, C_N = 59;
static constexpr unsigned WS_STATS = 16384;
static constexpr unsigned WS_CS    = WS_STATS + S * 8;
static constexpr unsigned WS_HT    = WS_CS + 16384;
static constexpr unsigned WS_WDT   = WS_HT + S * F * 2;
static constexpr unsigned WS_KVR   = WS_WDT + F * D * 2;
static constexpr unsigned WS_YT    = WS_KVR + S * D * 2;
static constexpr unsigned WS_END   = WS_YT + S * D * 2;
static constexpr int SC1 = 16;
#define PRE 8
#define HDELAY 3
#define TCH0 7
#define LD_AUX 16
static constexpr unsigned SPIN_CAP = 1u << 16;

DI float bflo(unsigned u) { return __uint_as_float(u << 16); }
DI float bfhi(unsigned u) { return __uint_as_float(u & 0xffff0000u); }
DI unsigned pk2(float a, float b) { bf16x2 r = {(__bf16)a, (__bf16)b}; return __builtin_bit_cast(unsigned, r); }
DI float rbf(float a) { return bflo(pk2(a, a)); }
template <int CTRL> DI float dpp_add(float v) {
    return v + __int_as_float(__builtin_amdgcn_update_dpp(0, __float_as_int(v), CTRL, 0xf, 0xf, true));
}
DI float sum8(float v) { v = dpp_add<0xB1>(v); v = dpp_add<0x4E>(v); return dpp_add<0x141>(v); }
DI float xor16_add(float v) { auto r = __builtin_amdgcn_permlane16_swap(__float_as_uint(v), __float_as_uint(v), false, false); return __uint_as_float(r[0]) + __uint_as_float(r[1]); }
DI float xor32_add(float v) { auto r = __builtin_amdgcn_permlane32_swap(__float_as_uint(v), __float_as_uint(v), false, false); return __uint_as_float(r[0]) + __uint_as_float(r[1]); }
DI float sum32(float v) { v = sum8(v); v = dpp_add<0x140>(v); return xor16_add(v); }
DI f32x16 mfma32(s16x8 a, s16x8 b, f32x16 c) {
    return __builtin_amdgcn_mfma_f32_32x32x16_bf16(__builtin_bit_cast(bf16x8, a), __builtin_bit_cast(bf16x8, b), c, 0, 0, 0);
}
DI f32x4 mfma16u(u32x4 a, u32x4 b, f32x4 c) {
    return __builtin_amdgcn_mfma_f32_16x16x32_bf16(__builtin_bit_cast(bf16x8, a), __builtin_bit_cast(bf16x8, b), c, 0, 0, 0);
}
DI f32x16 mfma32u(u32x4 a, u32x4 b, f32x16 c) { return mfma32(__builtin_bit_cast(s16x8, a), __builtin_bit_cast(s16x8, b), c); }
#define WAIT_V(n) asm volatile("s_waitcnt vmcnt(" #n ")" ::: "memory")
#define BAR() do { asm volatile("s_waitcnt lgkmcnt(0)" ::: "memory"); __builtin_amdgcn_s_barrier(); asm volatile("" ::: "memory"); } while (0)
#define SCHED() __builtin_amdgcn_sched_barrier(0)
DI __amdgpu_buffer_rsrc_t mkrs(const void* p, unsigned bytes) { return __builtin_amdgcn_make_buffer_rsrc((void*)p, 0, bytes, 0x00020000); }
DI gu32* cnt_ptr(char* ws, int i) { return (gu32*)(ws + SY_CNT + 128 * i); }
DI void poll_eq(gu32* p, unsigned want) {
    for (unsigned sp = 0; sp < SPIN_CAP; ++sp) { if (__hip_atomic_load(p, RLX) == want) break; __builtin_amdgcn_s_sleep(1); }
}
DI void poll_eq64(gu64* p, u64 want) {
    for (unsigned sp = 0; sp < SPIN_CAP; ++sp) { if (__hip_atomic_load(p, RLX) == want) break; __builtin_amdgcn_s_sleep(1); }
}
DI void poll_ge(gu32* p, unsigned want) {
    for (unsigned sp = 0; sp < SPIN_CAP; ++sp) { if (__hip_atomic_load(p, RLX) >= want) break; __builtin_amdgcn_s_sleep(1); }
}

struct Args {
    const u16* q; const u16* k; const u16* v; const u16* sk; const u16* ssm; const u16* Dv;
    const u16* wg; const u16* wu; const u16* wd;
    char* ws; u16* out;
};

static constexpr int G1_STAGE = 49152;
static constexpr int X_CS = 3 * G1_STAGE;
static constexpr int HC_STRIDE = 4160;
static constexpr int CH_BYTES = 8 * HC_STRIDE + 4608;
static constexpr int X_CRED = 2 * CH_BYTES;
static constexpr int RED_STRIDE = 272;
static constexpr int X_HC = X_CS + 1024;
static constexpr int X_SC = X_HC + 16;
static constexpr int LDS_BYTES = X_SC + 8192;
static constexpr int WWIN = 10;
static constexpr int HWIN = 8;

DI void dma16(const GAS char* sbase, unsigned voff, unsigned ldst) {
    unsigned keep;
    asm volatile("s_mov_b32 %0, m0\n\ts_mov_b32 m0, %3\n\ts_nop 0\n\tglobal_load_lds_dwordx4 %1, %2\n\ts_mov_b32 m0, %0"
                 : "=&s"(keep) : "v"(voff), "s"(sbase), "s"(ldst));
}

DI void service_block(const Args& a, LAS char* smem) {
    const int lane = threadIdx.x & 63, w = threadIdx.x >> 6;
    const __amdgpu_buffer_rsrc_t rs = mkrs(a.ws, WS_END);
    volatile LAS unsigned* stop = (volatile LAS unsigned*)smem;
    if (w == 0) {
        if (lane < C_N) __hip_atomic_store(cnt_ptr(a.ws, lane), 0u, RLX);
        if (lane == 0) *stop = 0u;
        WAIT_V(0);
    }
    BAR();
    if (w == 0) {
        for (unsigned it = 0; it < SPIN_CAP; ++it) {
            const unsigned c = __hip_atomic_load(cnt_ptr(a.ws, lane < C_N ? lane : 0), RLX);
            const bool ok = (lane >= C_CS && lane < C_N) ? (c >= 4u) : (lane < C_ST) ? (c >= 32u) : true;
            if (__all(ok)) break;
            __builtin_amdgcn_s_sleep(8);
        }
        if (lane == 0) *stop = 1u;
    } else if (w < 3) {
        for (int i = 0; i < (w - 1) * 4; ++i) __builtin_amdgcn_s_sleep(2);
        for (unsigned it = 0; it < SPIN_CAP; ++it) {
            const u32x4 m0 = __builtin_amdgcn_raw_buffer_load_b128(rs, SY_M + lane * 32, 0, SC1);
            const u32x4 m1 = __builtin_amdgcn_raw_buffer_load_b128(rs, SY_M + lane * 32 + 16, 0, SC1);
            __builtin_amdgcn_raw_buffer_store_b128(m0, rs, SY_A + lane * 32, 0, SC1);
            __builtin_amdgcn_raw_buffer_store_b128(m1, rs, SY_A + lane * 32 + 16, 0, SC1);
            if (*stop) break;
            __builtin_amdgcn_s_sleep(6);
        }
    }
}

DI u32x2 row_stats(const u32x4 (&xs)[7]) {
    float s0 = 0.f, s1 = 0.f;
#pragma unroll
    for (int i = 0; i < 7; ++i)
#pragma unroll
        for (int j = 0; j < 4; ++j) { s0 += bflo(xs[i][j]); s1 += bfhi(xs[i][j]); }
    float sum = s0 + s1;
    sum = sum8(sum);
    const float m = sum * (1.0f / D);
    float q0 = 0.f, q1 = 0.f;
#pragma unroll
    for (int i = 0; i < 7; ++i)
#pragma unroll
        for (int j = 0; j < 4; ++j) {
            const float d0 = bflo(xs[i][j]) - m, d1 = bfhi(xs[i][j]) - m;
            q0 = fmaf(d0, d0, q0); q1 = fmaf(d1, d1, q1);
        }
    float sq = q0 + q1;
    sq = sum8(sq);
    const float var = sq * (1.0f / D);
    const float mb = rbf(m), vb = rbf(var);
    const float den = rbf(sqrtf(rbf(vb + 1e-5f)));
    return u32x2{__float_as_uint(mb), __float_as_uint(1.0f / den)};
}

#define PSTAMP(i)
DI void prep_block(const Args& a, LAS char* smem) {
    const int tid = threadIdx.x, lane = tid & 63;
    const int w = __builtin_amdgcn_readfirstlane(tid >> 6);
    const int g = (blockIdx.x - NMAIN) * 8 + w;
    const __amdgpu_buffer_rsrc_t rs = mkrs(a.ws, WS_END);
    gu64* mbA = (gu64*)(a.ws + SY_A) + blockIdx.x;
    const u64 chal = __builtin_amdgcn_s_memrealtime();
    if (tid == 0) __hip_atomic_store((gu64*)(a.ws + SY_M) + blockIdx.x, chal, RLX);
    for (int i = 0; i < HDELAY; ++i) __builtin_amdgcn_s_sleep(16);
    const int hb = blockIdx.x - NMAIN;
    const u16* wsrc = (hb < 14 ? a.wg : a.wu) + (hb < 14 ? hb : hb - 14) * 128 + (tid & 15) * 8;
    u32x4 wr[14];
#pragma unroll
    for (int i = 0; i < 14; ++i) wr[i] = *(const u32x4*)(wsrc + (size_t)((tid >> 4) * 14 + i) * F);
    u32x4 xs[7];
    if (g < 32) {
        const int srow = (224 + g) * 8 + (lane >> 3);
#pragma unroll
        for (int i = 0; i < 7; ++i) xs[i] = *(const u32x4*)(a.q + (size_t)srow * D + ((lane & 7) + 8 * i) * 8);
    }
    SCHED();
    PSTAMP(1);
    float cs[8];
#pragma unroll
    for (int j = 0; j < 8; ++j) cs[j] = 0.f;
#pragma unroll
    for (int i = 0; i < 14; ++i)
#pragma unroll
        for (int e = 0; e < 4; ++e) { cs[2 * e] += bflo(wr[i][e]); cs[2 * e + 1] += bfhi(wr[i][e]); }
#pragma unroll
    for (int j = 0; j < 8; ++j) cs[j] = xor32_add(xor16_add(cs[j]));
    LAS f32x4* red = (LAS f32x4*)smem;
    if (lane < 16) { red[(w * 16 + lane) * 2] = f32x4{cs[0], cs[1], cs[2], cs[3]}; red[(w * 16 + lane) * 2 + 1] = f32x4{cs[4], cs[5], cs[6], cs[7]}; }
    if (g < 32) {
        const u32x2 o = row_stats(xs);
        if ((lane & 7) == 0) __builtin_amdgcn_raw_buffer_store_b64(o, rs, WS_STATS + (unsigned)((224 + g) * 8 + (lane >> 3)) * 8, 0, SC1);
    }
    BAR();
    if (tid < 32) {
        f32x4 s = red[tid];
#pragma unroll
        for (int ww = 1; ww < 8; ++ww) s += red[ww * 32 + tid];
        __builtin_amdgcn_raw_buffer_store_b128(__builtin_bit_cast(u32x4, s), rs, WS_CS + (unsigned)((hb < 14 ? 0 : 1792) + (hb < 14 ? hb : hb - 14) * 128 + tid * 4) * 4, 0, SC1);
    }
    PSTAMP(4);
    WAIT_V(0);
    PSTAMP(5);
    BAR();
    PSTAMP(6);
    if (w == 0) {
        if (lane == 0) poll_eq64(mbA, chal);
        const int cbh = hb < 14 ? hb : hb - 14;
        if (lane == 0 || (lane == 1 && hb < 4)) (void)__hip_atomic_fetch_add(cnt_ptr(a.ws, lane == 0 ? C_CS + (cbh >> 1) : C_HS), 1u, RLX);
    }
}

__global__ __launch_bounds__(512, 2) void fused_kernel(Args a) {
    extern __shared__ __attribute__((aligned(16))) char smem_[];
    LAS char* smem = (LAS char*)smem_;
    const int bid = blockIdx.x;
    STAMP_DECL
    STAMP(0);
    if (bid >= NMAIN) {
        if (bid == NMAIN + NPREP) service_block(a, smem); else prep_block(a, smem);
        STAMP(15); STAMP_FLUSH();
        return;
    }
    const int tid = threadIdx.x, lane = tid & 63;
    const int w = __builtin_amdgcn_readfirstlane(tid >> 6);
    const int xcd = bid & 7, idx = bid >> 3;
    const __amdgpu_buffer_rsrc_t rs = mkrs(a.ws, WS_END);
    const int rb = (xcd >> 1) * 4 + (idx & 3);
    const int cb = (xcd & 1) * 7 + (idx >> 2);
    const int rt = xcd * 4 + (idx & 3);
    const int ctile = idx >> 2;
    const int r = lane & 31, half = lane >> 5;
    const int rot = (2 * ctile + 3 * (idx & 3) + (idx & 2) / 2) % 14;
    const unsigned wdo = WS_WDT + (((unsigned)(ctile * 2) * 112 + 14 * w) * 64 + lane) * 16;
    const unsigned hto = WS_HT + (((unsigned)(rt * 2) * 112 + 14 * w) * 64 + lane) * 16;
    auto kso = [&](int i) { const int k = i + rot; return (unsigned)(k >= 14 ? k - 14 : k) * 1024u; };
    u32x4 wd[WWIN][2];
    auto wdload = [&](int n) {
        wd[(n >> 1) % WWIN][n & 1] = __builtin_amdgcn_raw_buffer_load_b128(rs, wdo + (n & 1) * 112 * 1024, kso(n >> 1), LD_AUX);
    };
    if (w >= 4) {
        const int lw = w - 4, lt = tid - 256;
        u32x4 xr[2], h0[2], h1[2];
        bool wd_early = false;
        auto taps = [&]() {
#pragma unroll
            for (int c2 = 0; c2 < 2; ++c2) {
                const u16* hs = a.ssm + (size_t)(2 * bid + c2) * S;
                h0[c2] = *(const u32x4*)(hs + (lt > 0 ? lt - 1 : 0) * 8);
                h1[c2] = *(const u32x4*)(hs + lt * 8);
            }
        };
        if (lw < 3) {
            const GAS char* gsrc; unsigned vo[2]; size_t cstep, istep; unsigned ldst;
            if (lw < 2) {
                gsrc = (const GAS char*)(lw ? a.wu : a.wg) + (size_t)cb * 256;
                vo[0] = vo[1] = (lane >> 4) * (F * 2) + (((lane & 15) ^ (((lane >> 4) << 2) | (lw << 1))) * 16);
                cstep = (size_t)64 * (F * 2); istep = (size_t)8 * (F * 2);
                ldst = 16384 + lw * 16384;
            } else {
                gsrc = (const GAS char*)a.q + (size_t)rb * 128 * D * 2;
#pragma unroll
                for (int e = 0; e < 2; ++e) { const int row = 8 * e + (lane >> 3); vo[e] = row * (D * 2) + (((lane & 7) ^ ((row >> 1) & 7)) * 16); }
                cstep = 128; istep = (size_t)16 * (D * 2);
                ldst = 0;
            }
            if (lw < 2) vo[1] += 4 * (F * 2);
            const unsigned lds0 = (unsigned)(uintptr_t)smem + ldst;
            auto pieces = [&](int c, int i0, int i1) {
#pragma unroll
                for (int i = i0; i < i1; ++i)
                    dma16(gsrc + c * cstep + (i >> 1) * istep, vo[i & 1], lds0 + (c % 3) * G1_STAGE + i * 1024);
            };
            auto stage = [&](int c) { pieces(c, 0, 16); };
            STAMP(24);
            stage(0);
            STAMP(25);
            pieces(1, 0, PRE);
            STAMP(26);
            asm volatile("s_waitcnt vmcnt(%0)" :: "n"(PRE) : "memory");
            BAR();
            STAMP(1);
            pieces(1, PRE, 16);
#pragma unroll
            for (int c = 0; c < 6; ++c) {
                if (c + 2 <= 6) stage(c + 2);
                asm volatile("" ::: "memory");
                SCHED();
                if (c == 4) taps();
                if (c == 5) {
                    wd_early = __builtin_amdgcn_readfirstlane(*(const LAS unsigned*)(smem + X_HC + 8)) != 0;
                    WAIT_V(4);
                    if (wd_early) {
#pragma unroll
                        for (int n = 0; n < 10; ++n) wdload(n);
                    }
                }
                SCHED();
                STAMP(16 + c);
                if (c == 4) WAIT_V(20);
                else if (c == 5) {}
                else WAIT_V(16);
                BAR();
                STAMP(2 + c);
            }
        } else {
            gu64* mbA = (gu64*)(a.ws + SY_A) + bid;
            const u64 chal = __builtin_amdgcn_s_memrealtime();
            if (lane == 0) __hip_atomic_store((gu64*)(a.ws + SY_M) + bid, chal, RLX);
            u32x4 wt[7], xs[7];
#pragma unroll
            for (int e = 0; e < 7; ++e) {
                const int t = 7 * bid + e, dt = t / 112, ks = t % 112;
                wt[e] = *(const u32x4*)(a.wd + (size_t)(16 * ks + (lane >> 2)) * D + 32 * dt + (lane & 3) * 8);
            }
#pragma unroll
            for (int i = 0; i < 7; ++i) xs[i] = *(const u32x4*)(a.q + (size_t)(8 * bid + (lane >> 3)) * D + ((lane & 7) + 8 * i) * 8);
            if (lane == 0) { *(LAS unsigned*)(smem + X_HC) = 0u; *(LAS unsigned*)(smem + X_HC + 4) = 0u; }
            SCHED();
            BAR();
            STAMP(1);
            u64 ans = __hip_atomic_load(mbA, RLX);
            {
                LAS char* sc = smem + X_SC;
                const int gi = lane >> 4, hf = gi >> 1, blk = gi & 1, q4 = (lane >> 2) & 3, p4 = lane & 3;
                const int ra = (4 * hf + q4) * 64 + (16 * blk + 4 * p4) * 2;
#pragma unroll
                for (int e = 0; e < 7; ++e) *(LAS u32x4*)(sc + e * 1024 + lane * 16) = wt[e];
                asm volatile("s_waitcnt lgkmcnt(0)" ::: "memory");
#pragma unroll
                for (int e = 0; e < 7; ++e) {
                    s16x4 lo = __builtin_amdgcn_ds_read_tr16_b64_v4i16((LAS s16x4*)(sc + e * 1024 + ra));
                    s16x4 hi = __builtin_amdgcn_ds_read_tr16_b64_v4i16((LAS s16x4*)(sc + e * 1024 + ra + 512));
                    s16x8 o = __builtin_shufflevector(lo, hi, 0, 1, 2, 3, 4, 5, 6, 7);
                    __builtin_amdgcn_raw_buffer_store_b128(__builtin_bit_cast(u32x4, o), rs, WS_WDT + (unsigned)(7 * bid + e) * 1024 + lane * 16, 0, SC1);
                }
            }
            STAMP(16);
            BAR();
            STAMP(2);
            if (ans != chal) { if (lane == 0) poll_eq64(mbA, chal); }
            if (lane == 0) (void)__hip_atomic_fetch_add(cnt_ptr(a.ws, C_KV + bid % 7), 1u, RLX);
            STAMP(22);
            {
                const u32x2 o = row_stats(xs);
                if ((lane & 7) == 0) __builtin_amdgcn_raw_buffer_store_b64(o, rs, WS_STATS + (unsigned)(8 * bid + (lane >> 3)) * 8, 0, SC1);
            }
            STAMP(17);
            BAR();
            STAMP(3);
            WAIT_V(0);
            if (lane < 2) (void)__hip_atomic_fetch_add(cnt_ptr(a.ws, lane == 0 ? C_WD + (bid >> 5) : C_ST + (bid >> 4)), 1u, RLX);
            STAMP(23);
            BAR();
            STAMP(4);
            taps();
            BAR();
            STAMP(5);
            const int ci = (lane == 1) ? (rb < 14 ? C_ST + rb : C_HS) : (lane == 2) ? C_WD + ctile : (lane == 3) ? C_CS + (cb >> 1) : C_KV + (bid >> 5);
            const unsigned want = (lane == 1) ? (rb < 14 ? 16u : 4u) : (lane == 3) ? 4u : 32u;
            unsigned cnt = __hip_atomic_load(cnt_ptr(a.ws, ci), RLX);
            wd_early = __builtin_amdgcn_readlane(cnt, 2) >= 32;
            if (lane == 0) *(LAS unsigned*)(smem + X_HC + 8) = wd_early ? 1u : 0u;
            BAR();
            STAMP(6);
            {
                if (!__all(cnt >= want)) cnt = __hip_atomic_load(cnt_ptr(a.ws, ci), RLX);
                if (wd_early) {
#pragma unroll
                    for (int n = 0; n < 10; ++n) wdload(n);
                }
                for (unsigned sp = 0; sp < SPIN_CAP; ++sp) {
                    if (__all(cnt >= want)) break;
                    __builtin_amdgcn_s_sleep(1);
                    cnt = __hip_atomic_load(cnt_ptr(a.ws, ci), RLX);
                }
            }
            STAMP(21);
            BAR();
            STAMP(7);
        }
        const u32x4 cv = __builtin_amdgcn_raw_buffer_load_b128(rs, WS_CS + (unsigned)((lane >> 5) * 1792 + cb * 128 + (lane & 31) * 4) * 4, 0, SC1);
#pragma unroll
        for (int c2 = 0; c2 < 2; ++c2)
            xr[c2] = __builtin_amdgcn_raw_buffer_load_b128(rs, WS_KVR + ((unsigned)(2 * bid + c2) * S + lt * 8) * 2, 0, SC1);
        if (!wd_early) {
#pragma unroll
            for (int n = 0; n < 10; ++n) wdload(n);
        }
#pragma unroll
        for (int n = 10; n < 20; ++n) wdload(n);
        if (lw == 0) *(LAS u32x4*)(smem + X_CS + lane * 16) = cv;
        BAR();
        STAMP(8);
#pragma unroll
        for (int c2 = 0; c2 < 2; ++c2) {
            LAS char* cbp = smem + c2 * CH_BYTES;
            u32x4 hh0 = h0[c2];
            if (lt == 0) hh0 = u32x4{0, 0, 0, 0};
            const unsigned d[8] = {hh0[0], hh0[1], hh0[2], hh0[3], h1[c2][0], h1[c2][1], h1[c2][2], h1[c2][3]};
            unsigned al[7];
#pragma unroll
            for (int i = 0; i < 7; ++i) al[i] = __builtin_amdgcn_alignbit(d[i + 1], d[i], 16);
            const int t = lt + 1;
#pragma unroll
            for (int sg = 0; sg < 8; ++sg) {
                const int sh = sg + 1;
                u32x4 o;
#pragma unroll
                for (int e = 0; e < 4; ++e) o[e] = (sh & 1) ? al[(sh >> 1) + e] : d[(sh >> 1) + e];
                *(LAS u32x4*)(cbp + sg * HC_STRIDE + t * 16) = o;
            }
            if (lt < 8) *(LAS u32x4*)(cbp + lt * HC_STRIDE) = u32x4{0, 0, 0, 0};
            *(LAS u32x4*)(cbp + 8 * HC_STRIDE + lt * 16) = xr[c2];
            if (lt < 32) *(LAS u32x4*)(cbp + 8 * HC_STRIDE + 4096 + lt * 16) = u32x4{0, 0, 0, 0};
        }
        STAMP(9);
    } else {
        const int s0 = (bid / 7) * 64 + (2 * w + half) * 8;
        const int dch = (bid % 7) * 64 + 2 * r;
        unsigned kr[11], vr[8];
#pragma unroll
        for (int i = 0; i < 11; ++i) {
            const int s = s0 - 3 + i;
            asm volatile("global_load_dword %0, %1, %2" : "=v"(kr[i]) : "v"((unsigned)((s < 0 ? 0 : s) * D + dch) * 2u), "s"((const GAS char*)a.k));
        }
#pragma unroll
        for (int i = 0; i < 8; ++i)
            asm volatile("global_load_dword %0, %1, %2" : "=v"(vr[i]) : "v"((unsigned)((s0 + i) * D + dch) * 2u), "s"((const GAS char*)a.v));
        u32x4 sk;
        asm volatile("global_load_dwordx4 %0, %1, %2" : "=v"(sk) : "v"((unsigned)dch * 8u), "s"((const GAS char*)a.sk));
        float ko[8];
        unsigned kpk[4];
        auto kv_part = [&](int c, int hp) {
            const float w0 = bflo(sk[2 * c]), w1 = bfhi(sk[2 * c]), w2 = bflo(sk[2 * c + 1]), w3 = bfhi(sk[2 * c + 1]);
            float kf[7];
#pragma unroll
            for (int i = 0; i < 7; ++i) {
                unsigned kk = kr[4 * hp + i];
                if (hp == 0 && i < 3) kk = (s0 == 0) ? 0u : kk;
                kf[i] = c ? bfhi(kk) : bflo(kk);
            }
#pragma unroll
            for (int i = 0; i < 4; ++i) {
                float acc = kf[i] * w3;
                acc = fmaf(kf[i + 1], w2, acc);
                acc = fmaf(kf[i + 2], w1, acc);
                acc = fmaf(kf[i + 3], w0, acc);
                ko[4 * hp + i] = acc * (c ? bfhi(vr[4 * hp + i]) : bflo(vr[4 * hp + i]));
            }
            kpk[3 - 2 * hp] = pk2(ko[4 * hp + 1], ko[4 * hp + 0]);
            kpk[2 - 2 * hp] = pk2(ko[4 * hp + 3], ko[4 * hp + 2]);
            if (hp == 1)
                __builtin_amdgcn_raw_buffer_store_b128(u32x4{kpk[0], kpk[1], kpk[2], kpk[3]}, rs, WS_KVR + ((unsigned)(dch + c) * S + (2040 - s0)) * 2, 0, SC1);
        };
        const int wm = w >> 1, wn = w & 1;
        unsigned xa[4], wa[4];
        {
            const int gi = lane >> 4, blk = gi & 1, q4 = (lane >> 2) & 3, p4 = lane & 3, gu = p4 >> 1;
#pragma unroll
            for (int t = 0; t < 4; ++t) xa[t] = (unsigned)(wm * 64 + r) * 128 + (((2 * t + half) ^ ((r >> 1) & 7)) * 16);
#pragma unroll
            for (int i = 0; i < 4; ++i) {
                const int pc = 2 * (4 * wn + i) + blk;
                wa[i] = 16384 + gu * 16384 + (8 * half + q4) * 256 + ((pc ^ ((q4 << 2) | (gu << 1))) * 16) + (p4 & 1) * 8;
            }
        }
        f32x16 acc[4][2];
#pragma unroll
        for (int i = 0; i < 4; ++i)
#pragma unroll
            for (int j = 0; j < 2; ++j)
#pragma unroll
                for (int e = 0; e < 16; ++e) acc[i][j][e] = 0.f;
        s16x8 xf[2][2], wf[2][4];
        auto rdfrag = [&](int ks) {
            const int c = ks >> 2, t = ks & 3, b = ks & 1;
            LAS char* sb = smem + (c % 3) * G1_STAGE;
#pragma unroll
            for (int j = 0; j < 2; ++j) xf[b][j] = *(const LAS s16x8*)(sb + xa[t] + j * 4096);
#pragma unroll
            for (int i = 0; i < 4; ++i) {
                s16x4 lo = __builtin_amdgcn_ds_read_tr16_b64_v4i16((LAS s16x4*)(sb + wa[i] + t * 4096));
                s16x4 hi = __builtin_amdgcn_ds_read_tr16_b64_v4i16((LAS s16x4*)(sb + wa[i] + t * 4096 + 1024));
                wf[b][i] = __builtin_shufflevector(lo, hi, 0, 1, 2, 3, 4, 5, 6, 7);
            }
        };
        float mean[2];
        asm volatile("s_waitcnt vmcnt(0)" : "+v"(kr[0]), "+v"(kr[1]), "+v"(kr[2]), "+v"(kr[3]), "+v"(kr[4]), "+v"(kr[5]), "+v"(kr[6]), "+v"(kr[7]), "+v"(kr[8]), "+v"(kr[9]), "+v"(kr[10]),
                     "+v"(vr[0]), "+v"(vr[1]), "+v"(vr[2]), "+v"(vr[3]), "+v"(vr[4]), "+v"(vr[5]), "+v"(vr[6]), "+v"(vr[7]), "+v"(sk));
        STAMP(24);
        kv_part(0, 0); kv_part(0, 1); kv_part(1, 0); kv_part(1, 1);
        STAMP(25);
        BAR();
        STAMP(1);
        unsigned tch[2] = {0u, 0u};
        rdfrag(0);
#pragma unroll
        for (int ks = 0; ks < 28; ++ks) {
            const int c = ks >> 2, t = ks & 3, b = ks & 1;
            if (t == 3 && c < 6) {
                if (c == 0) asm volatile("s_waitcnt vmcnt(0)" : "+v"(tch[0]), "+v"(tch[1]) :: "memory");
                if (c == 5) STAMP(6);
                BAR();
                if (c == 5) {
#pragma unroll
                    for (int j = 0; j < 2; ++j)
                        mean[j] = __uint_as_float(__builtin_amdgcn_raw_buffer_load_b32(rs, WS_STATS + (unsigned)(rb * 128 + (2 * wm + j) * 32 + r) * 8, 0, SC1));
                }
            }
            if (ks < 27) rdfrag(ks + 1);
#pragma unroll
            for (int i = 0; i < 4; ++i) {
                acc[i][0] = mfma32(wf[b][i], xf[b][0], acc[i][0]);
                acc[i][1] = mfma32(wf[b][i], xf[b][1], acc[i][1]);
            }
#pragma unroll
            for (int i = 0; i < 8; ++i) {
                __builtin_amdgcn_sched_group_barrier(0x008, 1, 0);
                if (i < 5) __builtin_amdgcn_sched_group_barrier(0x100, 2, 0);
            }
            SCHED();
        }
        STAMP(7);
        BAR();
        STAMP(8);
        const LAS float* cs = (const LAS float*)(smem + X_CS);
#pragma unroll
        for (int i = 0; i < 4; ++i) {
            const int ct = 4 * wn + i;
            f32x4 cv[4];
#pragma unroll
            for (int g = 0; g < 4; ++g) cv[g] = *(const LAS f32x4*)(cs + (g & 1) * 128 + 16 * ct + 8 * (g >> 1) + 4 * half);
#pragma unroll
            for (int j = 0; j < 2; ++j) {
                float hv[8];
#pragma unroll
                for (int e = 0; e < 4; ++e) {
                    float g0 = acc[i][j][e] - mean[j] * cv[0][e];
                    float u0 = acc[i][j][4 + e] - mean[j] * cv[1][e];
                    float g1 = acc[i][j][8 + e] - mean[j] * cv[2][e];
                    float u1 = acc[i][j][12 + e] - mean[j] * cv[3][e];
                    hv[e] = fmaxf(g0, 0.f) * u0;
                    hv[4 + e] = fmaxf(g1, 0.f) * u1;
                }
                u32x4 o = {pk2(hv[0], hv[1]), pk2(hv[2], hv[3]), pk2(hv[4], hv[5]), pk2(hv[6], hv[7])};
                const int T = rb * 4 + 2 * wm + j, KS = cb * 8 + ct;
                __builtin_amdgcn_raw_buffer_store_b128(o, rs, WS_HT + (((unsigned)T * 112 + KS) * 64 + lane) * 16, 0, SC1);
                wdload(2 * i + j);
                SCHED();
            }
        }
        STAMP(9);
    }
    const float Dval = bflo((unsigned)a.Dv[2 * bid + (w >> 2)]);
    BAR();
    STAMP(10);
    const int cw = w >> 2, qp = w & 3;
    f32x4 cacc[8];
#pragma unroll
    for (int c = 0; c < 8; ++c) cacc[c] = f32x4{0.f, 0.f, 0.f, 0.f};
    {
        LAS char* cbp = smem + cw * CH_BYTES;
        const int m = lane & 15, g = lane >> 4;
        const LAS char* ab0 = cbp + (m & 7) * HC_STRIDE + (8 * qp + g + (m >> 3)) * 16;
        const LAS char* gb0 = cbp + 8 * HC_STRIDE + (2032 - 256 * 7 + 64 * qp - 16 * m + 8 * g) * 2;
        u32x4 A[8], G[8], A2[8], G2[8];
#pragma unroll
        for (int p = 0; p < 8; ++p) {
            G[p] = *(const LAS u32x4*)(gb0 + (7 - p) * 512);
            A[p] = *(const LAS u32x4*)(ab0 + p * 512);
        }
        SCHED();
#pragma unroll
        for (int p = 0; p < 8; ++p) {
#pragma unroll
            for (int c = p; c < 8; ++c) cacc[c] = mfma16u(A[p], G[c - p], cacc[c]);
            A2[p] = *(const LAS u32x4*)(ab0 + 64 + p * 512);
            G2[7 - p] = *(const LAS u32x4*)(gb0 + 64 + p * 512);
            if (p < 6 && w < 4) { wdload(8 + 2 * p); wdload(9 + 2 * p); }
            if (p == 3 && w < 4) {
                WAIT_V(9);
                unsigned old = 0u;
                if (lane == 0) old = __hip_atomic_fetch_add((LAS unsigned*)(smem + X_HC), 1u, __ATOMIC_RELAXED, __HIP_MEMORY_SCOPE_WORKGROUP);
                if (lane == 0 && old == 3u) (void)__hip_atomic_fetch_add(cnt_ptr(a.ws, C_H + rb), 1u, RLX);
            }
            SCHED();
        }
#pragma unroll
        for (int mm = 7; mm >= 0; --mm) {
#pragma unroll
            for (int p = 0; p + mm < 8; ++p) cacc[p + mm] = mfma16u(A2[p], G2[mm], cacc[p + mm]);
        }
    }
    f32x4 keep[2];
    {
        LAS f32x4* red = (LAS f32x4*)(smem + X_CRED) + w * 512;
#pragma unroll
        for (int j = 0; j < 4; ++j) {
            if (qp != j) { red[(2 * j) * 64 + lane] = cacc[2 * j]; red[(2 * j + 1) * 64 + lane] = cacc[2 * j + 1]; }
            else { keep[0] = cacc[2 * j]; keep[1] = cacc[2 * j + 1]; }
        }
    }
    BAR();
    STAMP(11);
    {
        const LAS f32x4* red = (const LAS f32x4*)(smem + X_CRED) + cw * 2048;
        const int ap = lane & 15, g = lane >> 4;
        const LAS char* xrl = smem + cw * CH_BYTES + 8 * HC_STRIDE;
#pragma unroll
        for (int e2 = 0; e2 < 2; ++e2) {
            const int c = 2 * qp + e2;
            f32x4 s = keep[e2];
#pragma unroll
            for (int j = 1; j < 4; ++j) s += red[((qp + j) & 3) * 512 + c * 64 + lane];
            const int s0 = 256 * c + 16 * ap + 4 * g;
            const u32x2 kx = *(const LAS u32x2*)(xrl + (2044 - s0) * 2);
            const float kv0 = bfhi(kx[1]), kv1 = bflo(kx[1]), kv2 = bfhi(kx[0]), kv3 = bflo(kx[0]);
            const float y0 = rbf(s[0]) + rbf(kv0 * Dval), y1 = rbf(s[1]) + rbf(kv1 * Dval);
            const float y2 = rbf(s[2]) + rbf(kv2 * Dval), y3 = rbf(s[3]) + rbf(kv3 * Dval);
            u32x2 o = {pk2(y0, y1), pk2(y2, y3)};
            __builtin_amdgcn_raw_buffer_store_b64(o, rs, WS_YT + ((unsigned)(2 * bid + cw) * S + s0) * 2, 0, SC1);
        }
    }
    if (lane == 0) poll_ge(cnt_ptr(a.ws, C_H + (rt >> 1)), 14u);
    WAIT_V(0);
    {
        unsigned old = 0u;
        if (lane == 0) old = __hip_atomic_fetch_add((LAS unsigned*)(smem + X_HC + 4), 1u, __ATOMIC_RELAXED, __HIP_MEMORY_SCOPE_WORKGROUP);
        if (lane == 0 && old == 7u) (void)__hip_atomic_fetch_add(cnt_ptr(a.ws, C_Y + ((2 * bid) >> 6)), 1u, RLX);
    }
    STAMP(12);
    u32x4 hw[HWIN][2];
    auto hload = [&](int i) {
        hw[i % HWIN][0] = __builtin_amdgcn_raw_buffer_load_b128(rs, hto, kso(i), LD_AUX);
        hw[i % HWIN][1] = __builtin_amdgcn_raw_buffer_load_b128(rs, hto + 112 * 1024, kso(i), LD_AUX);
    };
#pragma unroll
    for (int i = 0; i < HWIN; ++i) hload(i);
    const int tp = tid >> 4, cq = tid & 15;
    const int tok0 = rt * 64 + 2 * tp, d0 = ctile * 64 + 4 * cq;
    u32x2 q0, q1; u32x4 stu;
    SCHED();
    BAR();
    f32x16 acc2[2][2];
#pragma unroll
    for (int i = 0; i < 2; ++i)
#pragma unroll
        for (int j = 0; j < 2; ++j)
#pragma unroll
            for (int e = 0; e < 16; ++e) acc2[i][j][e] = 0.f;
    unsigned yv[4];
#pragma unroll
    for (int i = 0; i < 14; ++i) {
        const u32x4 hf0 = hw[i % HWIN][0], hf1 = hw[i % HWIN][1];
        if (i + HWIN < 14) hload(i + HWIN);
        if (i == 12) {
            if (lane == 0) poll_ge(cnt_ptr(a.ws, C_Y + ctile), 32u);
            __builtin_amdgcn_fence(__ATOMIC_ACQUIRE, "wavefront");
#pragma unroll
            for (int j = 0; j < 4; ++j)
                yv[j] = __builtin_amdgcn_raw_buffer_load_b32(rs, WS_YT + (unsigned)(d0 + j) * 4096 + tok0 * 2, 0, SC1);
            q0 = *(const u32x2*)(a.q + (size_t)tok0 * D + d0);
            q1 = *(const u32x2*)(a.q + (size_t)(tok0 + 1) * D + d0);
            stu = __builtin_amdgcn_raw_buffer_load_b128(rs, WS_STATS + (unsigned)tok0 * 8, 0, SC1);
        }
        const u32x4 wf0 = wd[i % WWIN][0], wf1 = wd[i % WWIN][1];
        if (i + WWIN < 14) { wdload(2 * (i + WWIN)); wdload(2 * (i + WWIN) + 1); }
        acc2[0][0] = mfma32u(wf0, hf0, acc2[0][0]);
        acc2[0][1] = mfma32u(wf0, hf1, acc2[0][1]);
        acc2[1][0] = mfma32u(wf1, hf0, acc2[1][0]);
        acc2[1][1] = mfma32u(wf1, hf1, acc2[1][1]);
        SCHED();
    }
    STAMP(13);
    {
        LAS char* rbp = smem + w * (64 * RED_STRIDE);
#pragma unroll
        for (int i = 0; i < 2; ++i)
#pragma unroll
            for (int j = 0; j < 2; ++j)
#pragma unroll
                for (int g = 0; g < 4; ++g) {
                    f32x4 v = {acc2[i][j][4 * g], acc2[i][j][4 * g + 1], acc2[i][j][4 * g + 2], acc2[i][j][4 * g + 3]};
                    *(LAS f32x4*)(rbp + (j * 32 + r) * RED_STRIDE + (i * 32 + 8 * g + 4 * half) * 4) = v;
                }
    }
    BAR();
    {
        f32x4 o[2];
#pragma unroll
        for (int e = 0; e < 2; ++e) {
            const LAS char* rbp = smem + (2 * tp + e) * RED_STRIDE + cq * 16;
            f32x4 s = *(const LAS f32x4*)rbp;
#pragma unroll
            for (int ww = 1; ww < 8; ++ww) s += *(const LAS f32x4*)(rbp + ww * (64 * RED_STRIDE));
            o[e] = s;
        }
#pragma unroll
        for (int e = 0; e < 2; ++e) {
            const float mb = __uint_as_float(stu[2 * e]), rd = __uint_as_float(stu[2 * e + 1]);
            const u32x2 qq = e ? q1 : q0;
            float res[4];
#pragma unroll
            for (int j = 0; j < 4; ++j) {
                const float x = (j & 1) ? bfhi(qq[j >> 1]) : bflo(qq[j >> 1]);
                const float xn = rbf(rbf(x - mb) * rd);
                const float ov = rbf(o[e][j] * rd * rd);
                const float gg = rbf(ov * __builtin_amdgcn_rcpf(1.0f + __expf(-ov)));
                const float y = e ? bfhi(yv[j]) : bflo(yv[j]);
                const float sv = rbf(xn * y);
                res[j] = x + rbf(sv * gg);
            }
            u32x2 ov2 = {pk2(res[0], res[1]), pk2(res[2], res[3])};
            __builtin_amdgcn_raw_buffer_store_b64(ov2, mkrs(a.out, S * D * 2), (unsigned)((tok0 + e) * D + d0) * 2u, 0, SC1);
        }
    }
    STAMP(15);
    STAMP_FLUSH();
}

extern "C" void kernel_launch(void* const* d_in, const int* in_sizes, int n_in,
                              void* d_out, int out_size, void* d_ws, size_t ws_size,
                              hipStream_t stream) {
    (void)hipFuncSetAttribute((const void*)fused_kernel, hipFuncAttributeMaxDynamicSharedMemorySize, LDS_BYTES);
    Args a;
    a.q = (const u16*)d_in[0]; a.k = (const u16*)d_in[1]; a.v = (const u16*)d_in[2]; a.sk = (const u16*)d_in[3];
    a.ssm = (const u16*)d_in[4]; a.Dv = (const u16*)d_in[5];
    a.wg = (const u16*)d_in[7]; a.wu = (const u16*)d_in[8]; a.wd = (const u16*)d_in[9];
    a.ws = (char*)d_ws; a.out = (u16*)d_out;
    fused_kernel<<<NMAIN + NPREP + 1, 512, LDS_BYTES, stream>>>(a);
}
```

```cpp
#include <hip/hip_runtime.h>
#include <stdint.h>

typedef unsigned short u16;
typedef __attribute__((ext_vector_type(4))) short s16x4;
typedef __attribute__((ext_vector_type(8))) short s16x8;
typedef __attribute__((ext_vector_type(8))) __bf16 bf16x8;
typedef __attribute__((ext_vector_type(2))) __bf16 bf16x2;
typedef __attribute__((ext_vector_type(16))) float f32x16;
typedef __attribute__((ext_vector_type(4))) float f32x4;
typedef __attribute__((ext_vector_type(4))) unsigned u32x4;
typedef __attribute__((ext_vector_type(2))) unsigned u32x2;
#define GAS __attribute__((address_space(1)))
#define LAS __attribute__((address_space(3)))
#define DI __device__ __forceinline__
typedef GAS unsigned gu32;
typedef GAS unsigned long long gu64;
typedef unsigned long long u64;
#define RLX __ATOMIC_RELAXED, __HIP_MEMORY_SCOPE_AGENT

static constexpr int S = 2048, D = 448, F = 1792;
static constexpr int NMAIN = 224, NPREP = 28;
#define STAMP_DECL
#define STAMP(i)
#define STAMP_FLUSH()

static constexpr unsigned SY_M = 0, SY_A = 2048, SY_CNT = 4096;
static constexpr int C_KV = 0, C_ST = 7, C_HS = 21, C_WD = 22, C_H = 29, C_Y = 45, C_CS = 52, C_N = 59;
static constexpr unsigned WS_STATS = 16384;
static constexpr unsigned WS_CS    = WS_STATS + S * 8;
static constexpr unsigned WS_HT    = WS_CS + 16384;
static constexpr unsigned WS_WDT   = WS_HT + S * F * 2;
static constexpr unsigned WS_KVR   = WS_WDT + F * D * 2;
static constexpr unsigned WS_YT    = WS_KVR + S * D * 2;
static constexpr unsigned WS_END   = WS_YT + S * D * 2;
static constexpr int SC1 = 16;
#define CODE_LINES 296
#define PRE 8
#define HDELAY 3
#define TCH0 7
#define LD_AUX 16
static constexpr unsigned SPIN_CAP = 1u << 16;

DI float bflo(unsigned u) { return __uint_as_float(u << 16); }
DI float bfhi(unsigned u) { return __uint_as_float(u & 0xffff0000u); }
DI unsigned pk2(float a, float b) { bf16x2 r = {(__bf16)a, (__bf16)b}; return __builtin_bit_cast(unsigned, r); }
DI float rbf(float a) { return bflo(pk2(a, a)); }
template <int CTRL> DI float dpp_add(float v) {
    return v + __int_as_float(__builtin_amdgcn_update_dpp(0, __float_as_int(v), CTRL, 0xf, 0xf, true));
}
DI float sum8(float v) { v = dpp_add<0xB1>(v); v = dpp_add<0x4E>(v); return dpp_add<0x141>(v); }
DI float xor16_add(float v) { auto r = __builtin_amdgcn_permlane16_swap(__float_as_uint(v), __float_as_uint(v), false, false); return __uint_as_float(r[0]) + __uint_as_float(r[1]); }
DI float xor32_add(float v) { auto r = __builtin_amdgcn_permlane32_swap(__float_as_uint(v), __float_as_uint(v), false, false); return __uint_as_float(r[0]) + __uint_as_float(r[1]); }
DI float sum32(float v) { v = sum8(v); v = dpp_add<0x140>(v); return xor16_add(v); }
DI f32x16 mfma32(s16x8 a, s16x8 b, f32x16 c) {
    return __builtin_amdgcn_mfma_f32_32x32x16_bf16(__builtin_bit_cast(bf16x8, a), __builtin_bit_cast(bf16x8, b), c, 0, 0, 0);
}
DI f32x4 mfma16u(u32x4 a, u32x4 b, f32x4 c) {
    return __builtin_amdgcn_mfma_f32_16x16x32_bf16(__builtin_bit_cast(bf16x8, a), __builtin_bit_cast(bf16x8, b), c, 0, 0, 0);
}
DI f32x16 mfma32u(u32x4 a, u32x4 b, f32x16 c) { return mfma32(__builtin_bit_cast(s16x8, a), __builtin_bit_cast(s16x8, b), c); }
#define WAIT_V(n) asm volatile("s_waitcnt vmcnt(" #n ")" ::: "memory")
#define BAR() do { asm volatile("s_waitcnt lgkmcnt(0)" ::: "memory"); __builtin_amdgcn_s_barrier(); asm volatile("" ::: "memory"); } while (0)
#define SCHED() __builtin_amdgcn_sched_barrier(0)
DI __amdgpu_buffer_rsrc_t mkrs(const void* p, unsigned bytes) { return __builtin_amdgcn_make_buffer_rsrc((void*)p, 0, bytes, 0x00020000); }
DI gu32* cnt_ptr(char* ws, int i) { return (gu32*)(ws + SY_CNT + 128 * i); }
DI void poll_eq(gu32* p, unsigned want) {
    for (unsigned sp = 0; sp < SPIN_CAP; ++sp) { if (__hip_atomic_load(p, RLX) == want) break; __builtin_amdgcn_s_sleep(1); }
}
DI void poll_eq64(gu64* p, u64 want) {
    for (unsigned sp = 0; sp < SPIN_CAP; ++sp) { if (__hip_atomic_load(p, RLX) == want) break; __builtin_amdgcn_s_sleep(1); }
}
DI void poll_ge(gu32* p, unsigned want) {
    for (unsigned sp = 0; sp < SPIN_CAP; ++sp) { if (__hip_atomic_load(p, RLX) >= want) break; __builtin_amdgcn_s_sleep(1); }
}

struct Args {
    const u16* q; const u16* k; const u16* v; const u16* sk; const u16* ssm; const u16* Dv;
    const u16* wg; const u16* wu; const u16* wd;
    char* ws; u16* out;
};

static constexpr int G1_STAGE = 49152;
static constexpr int X_CS = 3 * G1_STAGE;
static constexpr int HC_STRIDE = 4160;
static constexpr int CH_BYTES = 8 * HC_STRIDE + 4608;
static constexpr int X_CRED = 2 * CH_BYTES;
static constexpr int RED_STRIDE = 272;
static constexpr int X_HC = X_CS + 1024;
static constexpr int X_SC = X_HC + 16;
static constexpr int LDS_BYTES = X_SC + 8192;
static constexpr int WWIN = 10;
static constexpr int HWIN = 8;

DI void dma16(const GAS char* sbase, unsigned voff, unsigned ldst) {
    unsigned keep;
    asm volatile("s_mov_b32 %0, m0\n\ts_mov_b32 m0, %3\n\ts_nop 0\n\tglobal_load_lds_dwordx4 %1, %2\n\ts_mov_b32 m0, %0"
                 : "=&s"(keep) : "v"(voff), "s"(sbase), "s"(ldst));
}

DI void service_block(const Args& a, LAS char* smem) {
    const int lane = threadIdx.x & 63, w = threadIdx.x >> 6;
    const __amdgpu_buffer_rsrc_t rs = mkrs(a.ws, WS_END);
    volatile LAS unsigned* stop = (volatile LAS unsigned*)smem;
    if (w == 0) {
        if (lane < C_N) __hip_atomic_store(cnt_ptr(a.ws, lane), 0u, RLX);
        if (lane == 0) *stop = 0u;
        WAIT_V(0);
    }
    BAR();
    if (w == 0) {
        for (unsigned it = 0; it < SPIN_CAP; ++it) {
            const unsigned c = __hip_atomic_load(cnt_ptr(a.ws, lane < C_N ? lane : 0), RLX);
            const bool ok = (lane >= C_CS && lane < C_N) ? (c >= 4u) : (lane < C_ST) ? (c >= 32u) : true;
            if (__all(ok)) break;
            __builtin_amdgcn_s_sleep(8);
        }
        if (lane == 0) *stop = 1u;
    } else if (w < 3) {
        for (int i = 0; i < (w - 1) * 4; ++i) __builtin_amdgcn_s_sleep(2);
        for (unsigned it = 0; it < SPIN_CAP; ++it) {
            const u32x4 m0 = __builtin_amdgcn_raw_buffer_load_b128(rs, SY_M + lane * 32, 0, SC1);
            const u32x4 m1 = __builtin_amdgcn_raw_buffer_load_b128(rs, SY_M + lane * 32 + 16, 0, SC1);
            __builtin_amdgcn_raw_buffer_store_b128(m0, rs, SY_A + lane * 32, 0, SC1);
            __builtin_amdgcn_raw_buffer_store_b128(m1, rs, SY_A + lane * 32 + 16, 0, SC1);
            if (*stop) break;
            __builtin_amdgcn_s_sleep(6);
        }
    }
}

DI u32x2 row_stats(const u32x4 (&xs)[7]) {
    float s0 = 0.f, s1 = 0.f;
#pragma unroll
    for (int i = 0; i < 7; ++i)
#pragma unroll
        for (int j = 0; j < 4; ++j) { s0 += bflo(xs[i][j]); s1 += bfhi(xs[i][j]); }
    float sum = s0 + s1;
    sum = sum8(sum);
    const float m = sum * (1.0f / D);
    float q0 = 0.f, q1 = 0.f;
#pragma unroll
    for (int i = 0; i < 7; ++i)
#pragma unroll
        for (int j = 0; j < 4; ++j) {
            const float d0 = bflo(xs[i][j]) - m, d1 = bfhi(xs[i][j]) - m;
            q0 = fmaf(d0, d0, q0); q1 = fmaf(d1, d1, q1);
        }
    float sq = q0 + q1;
    sq = sum8(sq);
    const float var = sq * (1.0f / D);
    const float mb = rbf(m), vb = rbf(var);
    const float den = rbf(sqrtf(rbf(vb + 1e-5f)));
    return u32x2{__float_as_uint(mb), __float_as_uint(1.0f / den)};
}

#define PSTAMP(i)
DI void prep_block(const Args& a, LAS char* smem) {
    const int tid = threadIdx.x, lane = tid & 63;
    const int w = __builtin_amdgcn_readfirstlane(tid >> 6);
    const int g = (blockIdx.x - NMAIN) * 8 + w;
    const __amdgpu_buffer_rsrc_t rs = mkrs(a.ws, WS_END);
    gu64* mbA = (gu64*)(a.ws + SY_A) + blockIdx.x;
    const u64 chal = __builtin_amdgcn_s_memrealtime();
    if (tid == 0) __hip_atomic_store((gu64*)(a.ws + SY_M) + blockIdx.x, chal, RLX);
    for (int i = 0; i < HDELAY; ++i) __builtin_amdgcn_s_sleep(16);
    const int hb = blockIdx.x - NMAIN;
    const u16* wsrc = (hb < 14 ? a.wg : a.wu) + (hb < 14 ? hb : hb - 14) * 128 + (tid & 15) * 8;
    u32x4 wr[14];
#pragma unroll
    for (int i = 0; i < 14; ++i) wr[i] = *(const u32x4*)(wsrc + (size_t)((tid >> 4) * 14 + i) * F);
    u32x4 xs[7];
    if (g < 32) {
        const int srow = (224 + g) * 8 + (lane >> 3);
#pragma unroll
        for (int i = 0; i < 7; ++i) xs[i] = *(const u32x4*)(a.q + (size_t)srow * D + ((lane & 7) + 8 * i) * 8);
    }
    SCHED();
    PSTAMP(1);
    float cs[8];
#pragma unroll
    for (int j = 0; j < 8; ++j) cs[j] = 0.f;
#pragma unroll
    for (int i = 0; i < 14; ++i)
#pragma unroll
        for (int e = 0; e < 4; ++e) { cs[2 * e] += bflo(wr[i][e]); cs[2 * e + 1] += bfhi(wr[i][e]); }
#pragma unroll
    for (int j = 0; j < 8; ++j) cs[j] = xor32_add(xor16_add(cs[j]));
    LAS f32x4* red = (LAS f32x4*)smem;
    if (lane < 16) { red[(w * 16 + lane) * 2] = f32x4{cs[0], cs[1], cs[2], cs[3]}; red[(w * 16 + lane) * 2 + 1] = f32x4{cs[4], cs[5], cs[6], cs[7]}; }
    if (g < 32) {
        const u32x2 o = row_stats(xs);
        if ((lane & 7) == 0) __builtin_amdgcn_raw_buffer_store_b64(o, rs, WS_STATS + (unsigned)((224 + g) * 8 + (lane >> 3)) * 8, 0, SC1);
    }
    BAR();
    if (tid < 32) {
        f32x4 s = red[tid];
#pragma unroll
        for (int ww = 1; ww < 8; ++ww) s += red[ww * 32 + tid];
        __builtin_amdgcn_raw_buffer_store_b128(__builtin_bit_cast(u32x4, s), rs, WS_CS + (unsigned)((hb < 14 ? 0 : 1792) + (hb < 14 ? hb : hb - 14) * 128 + tid * 4) * 4, 0, SC1);
    }
    PSTAMP(4);
    WAIT_V(0);
    PSTAMP(5);
    BAR();
    PSTAMP(6);
    if (w == 0) {
        if (lane == 0) poll_eq64(mbA, chal);
        const int cbh = hb < 14 ? hb : hb - 14;
        if (lane == 0 || (lane == 1 && hb < 4)) (void)__hip_atomic_fetch_add(cnt_ptr(a.ws, lane == 0 ? C_CS + (cbh >> 1) : C_HS), 1u, RLX);
    }
}

__global__ __launch_bounds__(512, 2) void fused_kernel(Args a) {
    extern __shared__ __attribute__((aligned(16))) char smem_[];
    LAS char* smem = (LAS char*)smem_;
    const int bid = blockIdx.x;
    u64 kernel_pc;
    asm volatile("s_getpc_b64 %0" : "=s"(kernel_pc));
    STAMP_DECL
    STAMP(0);
    if (bid >= NMAIN) {
        if (bid == NMAIN + NPREP) service_block(a, smem); else prep_block(a, smem);
        STAMP(15); STAMP_FLUSH();
        return;
    }
    const int tid = threadIdx.x, lane = tid & 63;
    const int w = __builtin_amdgcn_readfirstlane(tid >> 6);
    const int xcd = bid & 7, idx = bid >> 3;
    const __amdgpu_buffer_rsrc_t rs = mkrs(a.ws, WS_END);
    const int rb = (xcd >> 1) * 4 + (idx & 3);
    const int cb = (xcd & 1) * 7 + (idx >> 2);
    const int rt = xcd * 4 + (idx & 3);
    const int ctile = idx >> 2;
    const int r = lane & 31, half = lane >> 5;
    const int rot = (2 * ctile + 3 * (idx & 3) + (idx & 2) / 2) % 14;
    const unsigned wdo = WS_WDT + (((unsigned)(ctile * 2) * 112 + 14 * w) * 64 + lane) * 16;
    const unsigned hto = WS_HT + (((unsigned)(rt * 2) * 112 + 14 * w) * 64 + lane) * 16;
    auto kso = [&](int i) { const int k = i + rot; return (unsigned)(k >= 14 ? k - 14 : k) * 1024u; };
    u32x4 wd[WWIN][2];
    auto wdload = [&](int n) {
        wd[(n >> 1) % WWIN][n & 1] = __builtin_amdgcn_raw_buffer_load_b128(rs, wdo + (n & 1) * 112 * 1024, kso(n >> 1), LD_AUX);
    };
    if (w >= 4) {
        const int lw = w - 4, lt = tid - 256;
        u32x4 xr[2], h0[2], h1[2];
        bool wd_early = false;
        auto taps = [&]() {
#pragma unroll
            for (int c2 = 0; c2 < 2; ++c2) {
                const u16* hs = a.ssm + (size_t)(2 * bid + c2) * S;
                h0[c2] = *(const u32x4*)(hs + (lt > 0 ? lt - 1 : 0) * 8);
                h1[c2] = *(const u32x4*)(hs + lt * 8);
            }
        };
        if (lw < 3) {
            const GAS char* gsrc; unsigned vo[2]; size_t cstep, istep; unsigned ldst;
            if (lw < 2) {
                gsrc = (const GAS char*)(lw ? a.wu : a.wg) + (size_t)cb * 256;
                vo[0] = vo[1] = (lane >> 4) * (F * 2) + (((lane & 15) ^ (((lane >> 4) << 2) | (lw << 1))) * 16);
                cstep = (size_t)64 * (F * 2); istep = (size_t)8 * (F * 2);
                ldst = 16384 + lw * 16384;
            } else {
                gsrc = (const GAS char*)a.q + (size_t)rb * 128 * D * 2;
#pragma unroll
                for (int e = 0; e < 2; ++e) { const int row = 8 * e + (lane >> 3); vo[e] = row * (D * 2) + (((lane & 7) ^ ((row >> 1) & 7)) * 16); }
                cstep = 128; istep = (size_t)16 * (D * 2);
                ldst = 0;
            }
            if (lw < 2) vo[1] += 4 * (F * 2);
            const unsigned lds0 = (unsigned)(uintptr_t)smem + ldst;
            auto pieces = [&](int c, int i0, int i1) {
#pragma unroll
                for (int i = i0; i < i1; ++i)
                    dma16(gsrc + c * cstep + (i >> 1) * istep, vo[i & 1], lds0 + (c % 3) * G1_STAGE + i * 1024);
            };
            auto stage = [&](int c) { pieces(c, 0, 16); };
            STAMP(24);
            stage(0);
            STAMP(25);
            pieces(1, 0, PRE);
            STAMP(26);
            asm volatile("s_waitcnt vmcnt(%0)" :: "n"(PRE) : "memory");
            BAR();
            STAMP(1);
            pieces(1, PRE, 16);
#pragma unroll
            for (int c = 0; c < 6; ++c) {
                if (c + 2 <= 6) stage(c + 2);
                asm volatile("" ::: "memory");
                SCHED();
                if (c == 4) taps();
                if (c == 5) {
                    wd_early = __builtin_amdgcn_readfirstlane(*(const LAS unsigned*)(smem + X_HC + 8)) != 0;
                    WAIT_V(4);
                    if (wd_early) {
#pragma unroll
                        for (int n = 0; n < 10; ++n) wdload(n);
                    }
                }
                SCHED();
                STAMP(16 + c);
                if (c == 4) WAIT_V(20);
                else if (c == 5) {}
                else WAIT_V(16);
                BAR();
                STAMP(2 + c);
            }
        } else {
            unsigned cpf;
            {
                const u64 pc = kernel_pc & ~127ull;
                int n = idx * 11 + lane; n = n < CODE_LINES ? n : CODE_LINES - 1;
                asm volatile("global_load_dword %0, %1, off" : "=v"(cpf) : "v"(pc + (u64)(lane < 11 ? n : idx * 11) * 128ull));
            }
            gu64* mbA = (gu64*)(a.ws + SY_A) + bid;
            const u64 chal = __builtin_amdgcn_s_memrealtime();
            if (lane == 0) __hip_atomic_store((gu64*)(a.ws + SY_M) + bid, chal, RLX);
            u32x4 wt[7], xs[7];
#pragma unroll
            for (int e = 0; e < 7; ++e) {
                const int t = 7 * bid + e, dt = t / 112, ks = t % 112;
                wt[e] = *(const u32x4*)(a.wd + (size_t)(16 * ks + (lane >> 2)) * D + 32 * dt + (lane & 3) * 8);
            }
#pragma unroll
            for (int i = 0; i < 7; ++i) xs[i] = *(const u32x4*)(a.q + (size_t)(8 * bid + (lane >> 3)) * D + ((lane & 7) + 8 * i) * 8);
            if (lane == 0) { *(LAS unsigned*)(smem + X_HC) = 0u; *(LAS unsigned*)(smem + X_HC + 4) = 0u; }
            SCHED();
            BAR();
            STAMP(1);
            u64 ans = __hip_atomic_load(mbA, RLX);
            asm volatile("s_waitcnt vmcnt(15)" : "+v"(cpf));
            {
                LAS char* sc = smem + X_SC;
                const int gi = lane >> 4, hf = gi >> 1, blk = gi & 1, q4 = (lane >> 2) & 3, p4 = lane & 3;
                const int ra = (4 * hf + q4) * 64 + (16 * blk + 4 * p4) * 2;
#pragma unroll
                for (int e = 0; e < 7; ++e) *(LAS u32x4*)(sc + e * 1024 + lane * 16) = wt[e];
                asm volatile("s_waitcnt lgkmcnt(0)" ::: "memory");
#pragma unroll
                for (int e = 0; e < 7; ++e) {
                    s16x4 lo = __builtin_amdgcn_ds_read_tr16_b64_v4i16((LAS s16x4*)(sc + e * 1024 + ra));
                    s16x4 hi = __builtin_amdgcn_ds_read_tr16_b64_v4i16((LAS s16x4*)(sc + e * 1024 + ra + 512));
                    s16x8 o = __builtin_shufflevector(lo, hi, 0, 1, 2, 3, 4, 5, 6, 7);
                    __builtin_amdgcn_raw_buffer_store_b128(__builtin_bit_cast(u32x4, o), rs, WS_WDT + (unsigned)(7 * bid + e) * 1024 + lane * 16, 0, SC1);
                }
            }
            STAMP(16);
            BAR();
            STAMP(2);
            if (ans != chal) { if (lane == 0) poll_eq64(mbA, chal); }
            if (lane == 0) (void)__hip_atomic_fetch_add(cnt_ptr(a.ws, C_KV + bid % 7), 1u, RLX);
            STAMP(22);
            {
                const u32x2 o = row_stats(xs);
                if ((lane & 7) == 0) __builtin_amdgcn_raw_buffer_store_b64(o, rs, WS_STATS + (unsigned)(8 * bid + (lane >> 3)) * 8, 0, SC1);
            }
            STAMP(17);
            BAR();
            STAMP(3);
            WAIT_V(0);
            if (lane < 2) (void)__hip_atomic_fetch_add(cnt_ptr(a.ws, lane == 0 ? C_WD + (bid >> 5) : C_ST + (bid >> 4)), 1u, RLX);
            STAMP(23);
            BAR();
            STAMP(4);
            taps();
            BAR();
            STAMP(5);
            const int ci = (lane == 1) ? (rb < 14 ? C_ST + rb : C_HS) : (lane == 2) ? C_WD + ctile : (lane == 3) ? C_CS + (cb >> 1) : C_KV + (bid >> 5);
            const unsigned want = (lane == 1) ? (rb < 14 ? 16u : 4u) : (lane == 3) ? 4u : 32u;
            unsigned cnt = __hip_atomic_load(cnt_ptr(a.ws, ci), RLX);
            wd_early = __builtin_amdgcn_readlane(cnt, 2) >= 32;
            if (lane == 0) *(LAS unsigned*)(smem + X_HC + 8) = wd_early ? 1u : 0u;
            BAR();
            STAMP(6);
            {
                if (!__all(cnt >= want)) cnt = __hip_atomic_load(cnt_ptr(a.ws, ci), RLX);
                if (wd_early) {
#pragma unroll
                    for (int n = 0; n < 10; ++n) wdload(n);
                }
                for (unsigned sp = 0; sp < SPIN_CAP; ++sp) {
                    if (__all(cnt >= want)) break;
                    __builtin_amdgcn_s_sleep(1);
                    cnt = __hip_atomic_load(cnt_ptr(a.ws, ci), RLX);
                }
            }
            STAMP(21);
            BAR();
            STAMP(7);
        }
        const u32x4 cv = __builtin_amdgcn_raw_buffer_load_b128(rs, WS_CS + (unsigned)((lane >> 5) * 1792 + cb * 128 + (lane & 31) * 4) * 4, 0, SC1);
#pragma unroll
        for (int c2 = 0; c2 < 2; ++c2)
            xr[c2] = __builtin_amdgcn_raw_buffer_load_b128(rs, WS_KVR + ((unsigned)(2 * bid + c2) * S + lt * 8) * 2, 0, SC1);
        if (!wd_early) {
#pragma unroll
            for (int n = 0; n < 10; ++n) wdload(n);
        }
#pragma unroll
        for (int n = 10; n < 20; ++n) wdload(n);
        if (lw == 0) *(LAS u32x4*)(smem + X_CS + lane * 16) = cv;
        BAR();
        STAMP(8);
#pragma unroll
        for (int c2 = 0; c2 < 2; ++c2) {
            LAS char* cbp = smem + c2 * CH_BYTES;
            u32x4 hh0 = h0[c2];
            if (lt == 0) hh0 = u32x4{0, 0, 0, 0};
            const unsigned d[8] = {hh0[0], hh0[1], hh0[2], hh0[3], h1[c2][0], h1[c2][1], h1[c2][2], h1[c2][3]};
            unsigned al[7];
#pragma unroll
            for (int i = 0; i < 7; ++i) al[i] = __builtin_amdgcn_alignbit(d[i + 1], d[i], 16);
            const int t = lt + 1;
#pragma unroll
            for (int sg = 0; sg < 8; ++sg) {
                const int sh = sg + 1;
                u32x4 o;
#pragma unroll
                for (int e = 0; e < 4; ++e) o[e] = (sh & 1) ? al[(sh >> 1) + e] : d[(sh >> 1) + e];
                *(LAS u32x4*)(cbp + sg * HC_STRIDE + t * 16) = o;
            }
            if (lt < 8) *(LAS u32x4*)(cbp + lt * HC_STRIDE) = u32x4{0, 0, 0, 0};
            *(LAS u32x4*)(cbp + 8 * HC_STRIDE + lt * 16) = xr[c2];
            if (lt < 32) *(LAS u32x4*)(cbp + 8 * HC_STRIDE + 4096 + lt * 16) = u32x4{0, 0, 0, 0};
        }
        STAMP(9);
    } else {
        const int s0 = (bid / 7) * 64 + (2 * w + half) * 8;
        const int dch = (bid % 7) * 64 + 2 * r;
        unsigned kr[11], vr[8];
#pragma unroll
        for (int i = 0; i < 11; ++i) {
            const int s = s0 - 3 + i;
            asm volatile("global_load_dword %0, %1, %2" : "=v"(kr[i]) : "v"((unsigned)((s < 0 ? 0 : s) * D + dch) * 2u), "s"((const GAS char*)a.k));
        }
#pragma unroll
        for (int i = 0; i < 8; ++i)
            asm volatile("global_load_dword %0, %1, %2" : "=v"(vr[i]) : "v"((unsigned)((s0 + i) * D + dch) * 2u), "s"((const GAS char*)a.v));
        u32x4 sk;
        asm volatile("global_load_dwordx4 %0, %1, %2" : "=v"(sk) : "v"((unsigned)dch * 8u), "s"((const GAS char*)a.sk));
        float ko[8];
        unsigned kpk[4];
        auto kv_part = [&](int c, int hp) {
            const float w0 = bflo(sk[2 * c]), w1 = bfhi(sk[2 * c]), w2 = bflo(sk[2 * c + 1]), w3 = bfhi(sk[2 * c + 1]);
            float kf[7];
#pragma unroll
            for (int i = 0; i < 7; ++i) {
                unsigned kk = kr[4 * hp + i];
                if (hp == 0 && i < 3) kk = (s0 == 0) ? 0u : kk;
                kf[i] = c ? bfhi(kk) : bflo(kk);
            }
#pragma unroll
            for (int i = 0; i < 4; ++i) {
                float acc = kf[i] * w3;
                acc = fmaf(kf[i + 1], w2, acc);
                acc = fmaf(kf[i + 2], w1, acc);
                acc = fmaf(kf[i + 3], w0, acc);
                ko[4 * hp + i] = acc * (c ? bfhi(vr[4 * hp + i]) : bflo(vr[4 * hp + i]));
            }
            kpk[3 - 2 * hp] = pk2(ko[4 * hp + 1], ko[4 * hp + 0]);
            kpk[2 - 2 * hp] = pk2(ko[4 * hp + 3], ko[4 * hp + 2]);
            if (hp == 1)
                __builtin_amdgcn_raw_buffer_store_b128(u32x4{kpk[0], kpk[1], kpk[2], kpk[3]}, rs, WS_KVR + ((unsigned)(dch + c) * S + (2040 - s0)) * 2, 0, SC1);
        };
        const int wm = w >> 1, wn = w & 1;
        unsigned xa[4], wa[4];
        {
            const int gi = lane >> 4, blk = gi & 1, q4 = (lane >> 2) & 3, p4 = lane & 3, gu = p4 >> 1;
#pragma unroll
            for (int t = 0; t < 4; ++t) xa[t] = (unsigned)(wm * 64 + r) * 128 + (((2 * t + half) ^ ((r >> 1) & 7)) * 16);
#pragma unroll
            for (int i = 0; i < 4; ++i) {
                const int pc = 2 * (4 * wn + i) + blk;
                wa[i] = 16384 + gu * 16384 + (8 * half + q4) * 256 + ((pc ^ ((q4 << 2) | (gu << 1))) * 16) + (p4 & 1) * 8;
            }
        }
        f32x16 acc[4][2];
#pragma unroll
        for (int i = 0; i < 4; ++i)
#pragma unroll
            for (int j = 0; j < 2; ++j)
#pragma unroll
                for (int e = 0; e < 16; ++e) acc[i][j][e] = 0.f;
        s16x8 xf[2][2], wf[2][4];
        auto rdfrag = [&](int ks) {
            const int c = ks >> 2, t = ks & 3, b = ks & 1;
            LAS char* sb = smem + (c % 3) * G1_STAGE;
#pragma unroll
            for (int j = 0; j < 2; ++j) xf[b][j] = *(const LAS s16x8*)(sb + xa[t] + j * 4096);
#pragma unroll
            for (int i = 0; i < 4; ++i) {
                s16x4 lo = __builtin_amdgcn_ds_read_tr16_b64_v4i16((LAS s16x4*)(sb + wa[i] + t * 4096));
                s16x4 hi = __builtin_amdgcn_ds_read_tr16_b64_v4i16((LAS s16x4*)(sb + wa[i] + t * 4096 + 1024));
                wf[b][i] = __builtin_shufflevector(lo, hi, 0, 1, 2, 3, 4, 5, 6, 7);
            }
        };
        float mean[2];
        asm volatile("s_waitcnt vmcnt(0)" : "+v"(kr[0]), "+v"(kr[1]), "+v"(kr[2]), "+v"(kr[3]), "+v"(kr[4]), "+v"(kr[5]), "+v"(kr[6]), "+v"(kr[7]), "+v"(kr[8]), "+v"(kr[9]), "+v"(kr[10]),
                     "+v"(vr[0]), "+v"(vr[1]), "+v"(vr[2]), "+v"(vr[3]), "+v"(vr[4]), "+v"(vr[5]), "+v"(vr[6]), "+v"(vr[7]), "+v"(sk));
        STAMP(24);
        kv_part(0, 0); kv_part(0, 1); kv_part(1, 0); kv_part(1, 1);
        STAMP(25);
        BAR();
        STAMP(1);
        unsigned tch[2] = {0u, 0u};
        rdfrag(0);
#pragma unroll
        for (int ks = 0; ks < 28; ++ks) {
            const int c = ks >> 2, t = ks & 3, b = ks & 1;
            if (t == 3 && c < 6) {
                if (c == 0) asm volatile("s_waitcnt vmcnt(0)" : "+v"(tch[0]), "+v"(tch[1]) :: "memory");
                if (c == 5) STAMP(6);
                BAR();
                if (c == 5) {
#pragma unroll
                    for (int j = 0; j < 2; ++j)
                        mean[j] = __uint_as_float(__builtin_amdgcn_raw_buffer_load_b32(rs, WS_STATS + (unsigned)(rb * 128 + (2 * wm + j) * 32 + r) * 8, 0, SC1));
                }
            }
            if (ks < 27) rdfrag(ks + 1);
#pragma unroll
            for (int i = 0; i < 4; ++i) {
                acc[i][0] = mfma32(wf[b][i], xf[b][0], acc[i][0]);
                acc[i][1] = mfma32(wf[b][i], xf[b][1], acc[i][1]);
            }
#pragma unroll
            for (int i = 0; i < 8; ++i) {
                __builtin_amdgcn_sched_group_barrier(0x008, 1, 0);
                if (i < 5) __builtin_amdgcn_sched_group_barrier(0x100, 2, 0);
            }
            SCHED();
        }
        STAMP(7);
        BAR();
        STAMP(8);
        const LAS float* cs = (const LAS float*)(smem + X_CS);
#pragma unroll
        for (int i = 0; i < 4; ++i) {
            const int ct = 4 * wn + i;
            f32x4 cv[4];
#pragma unroll
            for (int g = 0; g < 4; ++g) cv[g] = *(const LAS f32x4*)(cs + (g & 1) * 128 + 16 * ct + 8 * (g >> 1) + 4 * half);
#pragma unroll
            for (int j = 0; j < 2; ++j) {
                float hv[8];
#pragma unroll
                for (int e = 0; e < 4; ++e) {
                    float g0 = acc[i][j][e] - mean[j] * cv[0][e];
                    float u0 = acc[i][j][4 + e] - mean[j] * cv[1][e];
                    float g1 = acc[i][j][8 + e] - mean[j] * cv[2][e];
                    float u1 = acc[i][j][12 + e] - mean[j] * cv[3][e];
                    hv[e] = fmaxf(g0, 0.f) * u0;
                    hv[4 + e] = fmaxf(g1, 0.f) * u1;
                }
                u32x4 o = {pk2(hv[0], hv[1]), pk2(hv[2], hv[3]), pk2(hv[4], hv[5]), pk2(hv[6], hv[7])};
                const int T = rb * 4 + 2 * wm + j, KS = cb * 8 + ct;
                __builtin_amdgcn_raw_buffer_store_b128(o, rs, WS_HT + (((unsigned)T * 112 + KS) * 64 + lane) * 16, 0, SC1);
                wdload(2 * i + j);
                SCHED();
            }
        }
        STAMP(9);
    }
    const float Dval = bflo((unsigned)a.Dv[2 * bid + (w >> 2)]);
    BAR();
    STAMP(10);
    const int cw = w >> 2, qp = w & 3;
    f32x4 cacc[8];
#pragma unroll
    for (int c = 0; c < 8; ++c) cacc[c] = f32x4{0.f, 0.f, 0.f, 0.f};
    {
        LAS char* cbp = smem + cw * CH_BYTES;
        const int m = lane & 15, g = lane >> 4;
        const LAS char* ab0 = cbp + (m & 7) * HC_STRIDE + (8 * qp + g + (m >> 3)) * 16;
        const LAS char* gb0 = cbp + 8 * HC_STRIDE + (2032 - 256 * 7 + 64 * qp - 16 * m + 8 * g) * 2;
        u32x4 A[8], G[8], A2[8], G2[8];
#pragma unroll
        for (int p = 0; p < 8; ++p) {
            G[p] = *(const LAS u32x4*)(gb0 + (7 - p) * 512);
            A[p] = *(const LAS u32x4*)(ab0 + p * 512);
        }
        SCHED();
#pragma unroll
        for (int p = 0; p < 8; ++p) {
#pragma unroll
            for (int c = p; c < 8; ++c) cacc[c] = mfma16u(A[p], G[c - p], cacc[c]);
            A2[p] = *(const LAS u32x4*)(ab0 + 64 + p * 512);
            G2[7 - p] = *(const LAS u32x4*)(gb0 + 64 + p * 512);
            if (p < 6 && w < 4) { wdload(8 + 2 * p); wdload(9 + 2 * p); }
            if (p == 3 && w < 4) {
                WAIT_V(9);
                unsigned old = 0u;
                if (lane == 0) old = __hip_atomic_fetch_add((LAS unsigned*)(smem + X_HC), 1u, __ATOMIC_RELAXED, __HIP_MEMORY_SCOPE_WORKGROUP);
                if (lane == 0 && old == 3u) (void)__hip_atomic_fetch_add(cnt_ptr(a.ws, C_H + rb), 1u, RLX);
            }
            SCHED();
        }
#pragma unroll
        for (int mm = 7; mm >= 0; --mm) {
#pragma unroll
            for (int p = 0; p + mm < 8; ++p) cacc[p + mm] = mfma16u(A2[p], G2[mm], cacc[p + mm]);
        }
    }
    f32x4 keep[2];
    {
        LAS f32x4* red = (LAS f32x4*)(smem + X_CRED) + w * 512;
#pragma unroll
        for (int j = 0; j < 4; ++j) {
            if (qp != j) { red[(2 * j) * 64 + lane] = cacc[2 * j]; red[(2 * j + 1) * 64 + lane] = cacc[2 * j + 1]; }
            else { keep[0] = cacc[2 * j]; keep[1] = cacc[2 * j + 1]; }
        }
    }
    BAR();
    STAMP(11);
    {
        const LAS f32x4* red = (const LAS f32x4*)(smem + X_CRED) + cw * 2048;
        const int ap = lane & 15, g = lane >> 4;
        const LAS char* xrl = smem + cw * CH_BYTES + 8 * HC_STRIDE;
#pragma unroll
        for (int e2 = 0; e2 < 2; ++e2) {
            const int c = 2 * qp + e2;
            f32x4 s = keep[e2];
#pragma unroll
            for (int j = 1; j < 4; ++j) s += red[((qp + j) & 3) * 512 + c * 64 + lane];
            const int s0 = 256 * c + 16 * ap + 4 * g;
            const u32x2 kx = *(const LAS u32x2*)(xrl + (2044 - s0) * 2);
            const float kv0 = bfhi(kx[1]), kv1 = bflo(kx[1]), kv2 = bfhi(kx[0]), kv3 = bflo(kx[0]);
            const float y0 = rbf(s[0]) + rbf(kv0 * Dval), y1 = rbf(s[1]) + rbf(kv1 * Dval);
            const float y2 = rbf(s[2]) + rbf(kv2 * Dval), y3 = rbf(s[3]) + rbf(kv3 * Dval);
            u32x2 o = {pk2(y0, y1), pk2(y2, y3)};
            __builtin_amdgcn_raw_buffer_store_b64(o, rs, WS_YT + ((unsigned)(2 * bid + cw) * S + s0) * 2, 0, SC1);
        }
    }
    if (lane == 0) poll_ge(cnt_ptr(a.ws, C_H + (rt >> 1)), 14u);
    WAIT_V(0);
    {
        unsigned old = 0u;
        if (lane == 0) old = __hip_atomic_fetch_add((LAS unsigned*)(smem + X_HC + 4), 1u, __ATOMIC_RELAXED, __HIP_MEMORY_SCOPE_WORKGROUP);
        if (lane == 0 && old == 7u) (void)__hip_atomic_fetch_add(cnt_ptr(a.ws, C_Y + ((2 * bid) >> 6)), 1u, RLX);
    }
    STAMP(12);
    u32x4 hw[HWIN][2];
    auto hload = [&](int i) {
        hw[i % HWIN][0] = __builtin_amdgcn_raw_buffer_load_b128(rs, hto, kso(i), LD_AUX);
        hw[i % HWIN][1] = __builtin_amdgcn_raw_buffer_load_b128(rs, hto + 112 * 1024, kso(i), LD_AUX);
    };
#pragma unroll
    for (int i = 0; i < HWIN; ++i) hload(i);
    const int tp = tid >> 4, cq = tid & 15;
    const int tok0 = rt * 64 + 2 * tp, d0 = ctile * 64 + 4 * cq;
    u32x2 q0, q1; u32x4 stu;
    SCHED();
    BAR();
    f32x16 acc2[2][2];
#pragma unroll
    for (int i = 0; i < 2; ++i)
#pragma unroll
        for (int j = 0; j < 2; ++j)
#pragma unroll
            for (int e = 0; e < 16; ++e) acc2[i][j][e] = 0.f;
    unsigned yv[4];
#pragma unroll
    for (int i = 0; i < 14; ++i) {
        const u32x4 hf0 = hw[i % HWIN][0], hf1 = hw[i % HWIN][1];
        if (i + HWIN < 14) hload(i + HWIN);
        if (i == 12) {
            if (lane == 0) poll_ge(cnt_ptr(a.ws, C_Y + ctile), 32u);
            __builtin_amdgcn_fence(__ATOMIC_ACQUIRE, "wavefront");
#pragma unroll
            for (int j = 0; j < 4; ++j)
                yv[j] = __builtin_amdgcn_raw_buffer_load_b32(rs, WS_YT + (unsigned)(d0 + j) * 4096 + tok0 * 2, 0, SC1);
            q0 = *(const u32x2*)(a.q + (size_t)tok0 * D + d0);
            q1 = *(const u32x2*)(a.q + (size_t)(tok0 + 1) * D + d0);
            stu = __builtin_amdgcn_raw_buffer_load_b128(rs, WS_STATS + (unsigned)tok0 * 8, 0, SC1);
        }
        const u32x4 wf0 = wd[i % WWIN][0], wf1 = wd[i % WWIN][1];
        if (i + WWIN < 14) { wdload(2 * (i + WWIN)); wdload(2 * (i + WWIN) + 1); }
        acc2[0][0] = mfma32u(wf0, hf0, acc2[0][0]);
        acc2[0][1] = mfma32u(wf0, hf1, acc2[0][1]);
        acc2[1][0] = mfma32u(wf1, hf0, acc2[1][0]);
        acc2[1][1] = mfma32u(wf1, hf1, acc2[1][1]);
        SCHED();
    }
    STAMP(13);
    {
        LAS char* rbp = smem + w * (64 * RED_STRIDE);
#pragma unroll
        for (int i = 0; i < 2; ++i)
#pragma unroll
            for (int j = 0; j < 2; ++j)
#pragma unroll
                for (int g = 0; g < 4; ++g) {
                    f32x4 v = {acc2[i][j][4 * g], acc2[i][j][4 * g + 1], acc2[i][j][4 * g + 2], acc2[i][j][4 * g + 3]};
                    *(LAS f32x4*)(rbp + (j * 32 + r) * RED_STRIDE + (i * 32 + 8 * g + 4 * half) * 4) = v;
                }
    }
    BAR();
    {
        f32x4 o[2];
#pragma unroll
        for (int e = 0; e < 2; ++e) {
            const LAS char* rbp = smem + (2 * tp + e) * RED_STRIDE + cq * 16;
            f32x4 s = *(const LAS f32x4*)rbp;
#pragma unroll
            for (int ww = 1; ww < 8; ++ww) s += *(const LAS f32x4*)(rbp + ww * (64 * RED_STRIDE));
            o[e] = s;
        }
#pragma unroll
        for (int e = 0; e < 2; ++e) {
            const float mb = __uint_as_float(stu[2 * e]), rd = __uint_as_float(stu[2 * e + 1]);
            const u32x2 qq = e ? q1 : q0;
            float res[4];
#pragma unroll
            for (int j = 0; j < 4; ++j) {
                const float x = (j & 1) ? bfhi(qq[j >> 1]) : bflo(qq[j >> 1]);
                const float xn = rbf(rbf(x - mb) * rd);
                const float ov = rbf(o[e][j] * rd * rd);
                const float gg = rbf(ov * __builtin_amdgcn_rcpf(1.0f + __expf(-ov)));
                const float y = e ? bfhi(yv[j]) : bflo(yv[j]);
                const float sv = rbf(xn * y);
                res[j] = x + rbf(sv * gg);
            }
            u32x2 ov2 = {pk2(res[0], res[1]), pk2(res[2], res[3])};
            __builtin_amdgcn_raw_buffer_store_b64(ov2, mkrs(a.out, S * D * 2), (unsigned)((tok0 + e) * D + d0) * 2u, 0, SC1);
        }
    }
    STAMP(15);
    STAMP_FLUSH();
}

extern "C" void kernel_launch(void* const* d_in, const int* in_sizes, int n_in,
                              void* d_out, int out_size, void* d_ws, size_t ws_size,
                              hipStream_t stream) {
    (void)hipFuncSetAttribute((const void*)fused_kernel, hipFuncAttributeMaxDynamicSharedMemorySize, LDS_BYTES);
    Args a;
    a.q = (const u16*)d_in[0]; a.k = (const u16*)d_in[1]; a.v = (const u16*)d_in[2]; a.sk = (const u16*)d_in[3];
    a.ssm = (const u16*)d_in[4]; a.Dv = (const u16*)d_in[5];
    a.wg = (const u16*)d_in[7]; a.wu = (const u16*)d_in[8]; a.wd = (const u16*)d_in[9];
    a.ws = (char*)d_ws; a.out = (u16*)d_out;
    fused_kernel<<<NMAIN + NPREP + 1, 512, LDS_BYTES, stream>>>(a);
}
```

```cpp
#include <hip/hip_runtime.h>
#include <stdint.h>

typedef unsigned short u16;
typedef __attribute__((ext_vector_type(4))) short s16x4;
typedef __attribute__((ext_vector_type(8))) short s16x8;
typedef __attribute__((ext_vector_type(8))) __bf16 bf16x8;
typedef __attribute__((ext_vector_type(2))) __bf16 bf16x2;
typedef __attribute__((ext_vector_type(16))) float f32x16;
typedef __attribute__((ext_vector_type(4))) float f32x4;
typedef __attribute__((ext_vector_type(4))) unsigned u32x4;
typedef __attribute__((ext_vector_type(2))) unsigned u32x2;
#define GAS __attribute__((address_space(1)))
#define LAS __attribute__((address_space(3)))
#define DI __device__ __forceinline__
typedef GAS unsigned gu32;
typedef GAS unsigned long long gu64;
typedef unsigned long long u64;
#define RLX __ATOMIC_RELAXED, __HIP_MEMORY_SCOPE_AGENT

static constexpr int S = 2048, D = 448, F = 1792;
static constexpr int NMAIN = 224, NPREP = 28;
#define STAMP_DECL
#define STAMP(i)
#define STAMP_FLUSH()

static constexpr unsigned SY_M = 0, SY_A = 2048, SY_CNT = 4096;
static constexpr int C_KV = 0, C_ST = 7, C_HS = 21, C_WD = 22, C_H = 29, C_Y = 45, C_CS = 52, C_N = 59;
static constexpr unsigned WS_STATS = 16384;
static constexpr unsigned WS_CS    = WS_STATS + S * 8;
static constexpr unsigned WS_HT    = WS_CS + 16384;
static constexpr unsigned WS_WDT   = WS_HT + S * F * 2;
static constexpr unsigned WS_KVR   = WS_WDT + F * D * 2;
static constexpr unsigned WS_YT    = WS_KVR + S * D * 2;
static constexpr unsigned WS_END   = WS_YT + S * D * 2;
static constexpr int SC1 = 16;
#define ROT1 0
#define PAGE_TOUCH 0
#define CODE_LINES 0
#define PRE 8
#define HDELAY 3
#define TCH0 7
#define LD_AUX 16
static constexpr unsigned SPIN_CAP = 1u << 16;

DI float bflo(unsigned u) { return __uint_as_float(u << 16); }
DI float bfhi(unsigned u) { return __uint_as_float(u & 0xffff0000u); }
DI unsigned pk2(float a, float b) { bf16x2 r = {(__bf16)a, (__bf16)b}; return __builtin_bit_cast(unsigned, r); }
DI float rbf(float a) { return bflo(pk2(a, a)); }
template <int CTRL> DI float dpp_add(float v) {
    return v + __int_as_float(__builtin_amdgcn_update_dpp(0, __float_as_int(v), CTRL, 0xf, 0xf, true));
}
DI float sum8(float v) { v = dpp_add<0xB1>(v); v = dpp_add<0x4E>(v); return dpp_add<0x141>(v); }
DI float xor16_add(float v) { auto r = __builtin_amdgcn_permlane16_swap(__float_as_uint(v), __float_as_uint(v), false, false); return __uint_as_float(r[0]) + __uint_as_float(r[1]); }
DI float xor32_add(float v) { auto r = __builtin_amdgcn_permlane32_swap(__float_as_uint(v), __float_as_uint(v), false, false); return __uint_as_float(r[0]) + __uint_as_float(r[1]); }
DI float sum32(float v) { v = sum8(v); v = dpp_add<0x140>(v); return xor16_add(v); }
DI f32x16 mfma32(s16x8 a, s16x8 b, f32x16 c) {
    return __builtin_amdgcn_mfma_f32_32x32x16_bf16(__builtin_bit_cast(bf16x8, a), __builtin_bit_cast(bf16x8, b), c, 0, 0, 0);
}
DI f32x4 mfma16u(u32x4 a, u32x4 b, f32x4 c) {
    return __builtin_amdgcn_mfma_f32_16x16x32_bf16(__builtin_bit_cast(bf16x8, a), __builtin_bit_cast(bf16x8, b), c, 0, 0, 0);
}
DI f32x16 mfma32u(u32x4 a, u32x4 b, f32x16 c) { return mfma32(__builtin_bit_cast(s16x8, a), __builtin_bit_cast(s16x8, b), c); }
#define WAIT_V(n) asm volatile("s_waitcnt vmcnt(" #n ")" ::: "memory")
#define BAR() do { asm volatile("s_waitcnt lgkmcnt(0)" ::: "memory"); __builtin_amdgcn_s_barrier(); asm volatile("" ::: "memory"); } while (0)
#define SCHED() __builtin_amdgcn_sched_barrier(0)
DI __amdgpu_buffer_rsrc_t mkrs(const void* p, unsigned bytes) { return __builtin_amdgcn_make_buffer_rsrc((void*)p, 0, bytes, 0x00020000); }
DI gu32* cnt_ptr(char* ws, int i) { return (gu32*)(ws + SY_CNT + 128 * i); }
DI void poll_eq(gu32* p, unsigned want) {
    for (unsigned sp = 0; sp < SPIN_CAP; ++sp) { if (__hip_atomic_load(p, RLX) == want) break; __builtin_amdgcn_s_sleep(1); }
}
DI void poll_eq64(gu64* p, u64 want) {
    for (unsigned sp = 0; sp < SPIN_CAP; ++sp) { if (__hip_atomic_load(p, RLX) == want) break; __builtin_amdgcn_s_sleep(1); }
}
DI void poll_ge(gu32* p, unsigned want) {
    for (unsigned sp = 0; sp < SPIN_CAP; ++sp) { if (__hip_atomic_load(p, RLX) >= want) break; __builtin_amdgcn_s_sleep(1); }
}

struct Args {
    const u16* q; const u16* k; const u16* v; const u16* sk; const u16* ssm; const u16* Dv;
    const u16* wg; const u16* wu; const u16* wd;
    char* ws; u16* out;
};

static constexpr int G1_STAGE = 49152;
static constexpr int X_CS = 3 * G1_STAGE;
static constexpr int HC_STRIDE = 4160;
static constexpr int CH_BYTES = 8 * HC_STRIDE + 4608;
static constexpr int X_CRED = 2 * CH_BYTES;
static constexpr int RED_STRIDE = 272;
static constexpr int X_HC = X_CS + 1024;
static constexpr int X_SC = X_HC + 16;
static constexpr int LDS_BYTES = X_SC + 8192;
static constexpr int WWIN = 14;
static constexpr int HWIN = 6;

DI void dma16(const GAS char* sbase, unsigned voff, unsigned ldst) {
    unsigned keep;
    asm volatile("s_mov_b32 %0, m0\n\ts_mov_b32 m0, %3\n\ts_nop 0\n\tglobal_load_lds_dwordx4 %1, %2\n\ts_mov_b32 m0, %0"
                 : "=&s"(keep) : "v"(voff), "s"(sbase), "s"(ldst));
}

DI void service_block(const Args& a, LAS char* smem) {
    const int lane = threadIdx.x & 63, w = threadIdx.x >> 6;
    const __amdgpu_buffer_rsrc_t rs = mkrs(a.ws, WS_END);
    volatile LAS unsigned* stop = (volatile LAS unsigned*)smem;
    if (w == 0) {
        if (lane < C_N) __hip_atomic_store(cnt_ptr(a.ws, lane), 0u, RLX);
        if (lane == 0) *stop = 0u;
        WAIT_V(0);
    }
    BAR();
    if (w == 0) {
        for (unsigned it = 0; it < SPIN_CAP; ++it) {
            const unsigned c = __hip_atomic_load(cnt_ptr(a.ws, lane < C_N ? lane : 0), RLX);
            const bool ok = (lane >= C_CS && lane < C_N) ? (c >= 4u) : (lane < C_ST) ? (c >= 32u) : true;
            if (__all(ok)) break;
            __builtin_amdgcn_s_sleep(8);
        }
        if (lane == 0) *stop = 1u;
    } else if (w < 3) {
        for (int i = 0; i < (w - 1) * 4; ++i) __builtin_amdgcn_s_sleep(2);
        for (unsigned it = 0; it < SPIN_CAP; ++it) {
            const u32x4 m0 = __builtin_amdgcn_raw_buffer_load_b128(rs, SY_M + lane * 32, 0, SC1);
            const u32x4 m1 = __builtin_amdgcn_raw_buffer_load_b128(rs, SY_M + lane * 32 + 16, 0, SC1);
            __builtin_amdgcn_raw_buffer_store_b128(m0, rs, SY_A + lane * 32, 0, SC1);
            __builtin_amdgcn_raw_buffer_store_b128(m1, rs, SY_A + lane * 32 + 16, 0, SC1);
            if (*stop) break;
            __builtin_amdgcn_s_sleep(6);
        }
    }
}

DI u32x2 row_stats(const u32x4 (&xs)[7]) {
    float s0 = 0.f, s1 = 0.f;
#pragma unroll
    for (int i = 0; i < 7; ++i)
#pragma unroll
        for (int j = 0; j < 4; ++j) { s0 += bflo(xs[i][j]); s1 += bfhi(xs[i][j]); }
    float sum = s0 + s1;
    sum = sum8(sum);
    const float m = sum * (1.0f / D);
    float q0 = 0.f, q1 = 0.f;
#pragma unroll
    for (int i = 0; i < 7; ++i)
#pragma unroll
        for (int j = 0; j < 4; ++j) {
            const float d0 = bflo(xs[i][j]) - m, d1 = bfhi(xs[i][j]) - m;
            q0 = fmaf(d0, d0, q0); q1 = fmaf(d1, d1, q1);
        }
    float sq = q0 + q1;
    sq = sum8(sq);
    const float var = sq * (1.0f / D);
    const float mb = rbf(m), vb = rbf(var);
    const float den = rbf(sqrtf(rbf(vb + 1e-5f)));
    return u32x2{__float_as_uint(mb), __float_as_uint(1.0f / den)};
}

#define PSTAMP(i)
DI void prep_block(const Args& a, LAS char* smem) {
    const int tid = threadIdx.x, lane = tid & 63;
    const int w = __builtin_amdgcn_readfirstlane(tid >> 6);
    const int g = (blockIdx.x - NMAIN) * 8 + w;
    const __amdgpu_buffer_rsrc_t rs = mkrs(a.ws, WS_END);
    gu64* mbA = (gu64*)(a.ws + SY_A) + blockIdx.x;
    const u64 chal = __builtin_amdgcn_s_memrealtime();
    if (tid == 0) __hip_atomic_store((gu64*)(a.ws + SY_M) + blockIdx.x, chal, RLX);
    for (int i = 0; i < HDELAY; ++i) __builtin_amdgcn_s_sleep(16);
    const int hb = blockIdx.x - NMAIN;
    const u16* wsrc = (hb < 14 ? a.wg : a.wu) + (hb < 14 ? hb : hb - 14) * 128 + (tid & 15) * 8;
    u32x4 wr[14];
#pragma unroll
    for (int i = 0; i < 14; ++i) wr[i] = *(const u32x4*)(wsrc + (size_t)((tid >> 4) * 14 + i) * F);
    u32x4 xs[7];
    if (g < 32) {
        const int srow = (224 + g) * 8 + (lane >> 3);
#pragma unroll
        for (int i = 0; i < 7; ++i) xs[i] = *(const u32x4*)(a.q + (size_t)srow * D + ((lane & 7) + 8 * i) * 8);
    }
    SCHED();
    PSTAMP(1);
    float cs[8];
#pragma unroll
    for (int j = 0; j < 8; ++j) cs[j] = 0.f;
#pragma unroll
    for (int i = 0; i < 14; ++i)
#pragma unroll
        for (int e = 0; e < 4; ++e) { cs[2 * e] += bflo(wr[i][e]); cs[2 * e + 1] += bfhi(wr[i][e]); }
#pragma unroll
    for (int j = 0; j < 8; ++j) cs[j] = xor32_add(xor16_add(cs[j]));
    LAS f32x4* red = (LAS f32x4*)smem;
    if (lane < 16) { red[(w * 16 + lane) * 2] = f32x4{cs[0], cs[1], cs[2], cs[3]}; red[(w * 16 + lane) * 2 + 1] = f32x4{cs[4], cs[5], cs[6], cs[7]}; }
    if (g < 32) {
        const u32x2 o = row_stats(xs);
        if ((lane & 7) == 0) __builtin_amdgcn_raw_buffer_store_b64(o, rs, WS_STATS + (unsigned)((224 + g) * 8 + (lane >> 3)) * 8, 0, SC1);
    }
    BAR();
    if (tid < 32) {
        f32x4 s = red[tid];
#pragma unroll
        for (int ww = 1; ww < 8; ++ww) s += red[ww * 32 + tid];
        __builtin_amdgcn_raw_buffer_store_b128(__builtin_bit_cast(u32x4, s), rs, WS_CS + (unsigned)((hb < 14 ? 0 : 1792) + (hb < 14 ? hb : hb - 14) * 128 + tid * 4) * 4, 0, SC1);
    }
    PSTAMP(4);
    WAIT_V(0);
    PSTAMP(5);
    BAR();
    PSTAMP(6);
    if (w == 0) {
        if (lane == 0) poll_eq64(mbA, chal);
        const int cbh = hb < 14 ? hb : hb - 14;
        if (lane == 0 || (lane == 1 && hb < 4)) (void)__hip_atomic_fetch_add(cnt_ptr(a.ws, lane == 0 ? C_CS + (cbh >> 1) : C_HS), 1u, RLX);
    }
}

__global__ __launch_bounds__(512, 2) void fused_kernel(Args a) {
    extern __shared__ __attribute__((aligned(16))) char smem_[];
    LAS char* smem = (LAS char*)smem_;
    const int bid = blockIdx.x;
    u64 kernel_pc;
    asm volatile("s_getpc_b64 %0" : "=s"(kernel_pc));
    STAMP_DECL
    STAMP(0);
    if (bid >= NMAIN) {
        if (bid == NMAIN + NPREP) service_block(a, smem); else prep_block(a, smem);
        STAMP(15); STAMP_FLUSH();
        return;
    }
    const int tid = threadIdx.x, lane = tid & 63;
    const int w = __builtin_amdgcn_readfirstlane(tid >> 6);
    const int xcd = bid & 7, idx = bid >> 3;
    const __amdgpu_buffer_rsrc_t rs = mkrs(a.ws, WS_END);
    const int rb = (xcd >> 1) * 4 + (idx & 3);
    const int cb = (xcd & 1) * 7 + (idx >> 2);
    const int rt = xcd * 4 + (idx & 3);
    const int ctile = idx >> 2;
    const int r = lane & 31, half = lane >> 5;
    const int rot = (2 * ctile + 3 * (idx & 3) + (idx & 2) / 2) % 14;
    const unsigned wdo = WS_WDT + (((unsigned)(ctile * 2) * 112 + 14 * w) * 64 + lane) * 16;
    const unsigned hto = WS_HT + (((unsigned)(rt * 2) * 112 + 14 * w) * 64 + lane) * 16;
    auto kso = [&](int i) { const int k = i + rot; return (unsigned)(k >= 14 ? k - 14 : k) * 1024u; };
    u32x4 wd[WWIN][2];
    auto wdload = [&](int n) {
        wd[(n >> 1) % WWIN][n & 1] = __builtin_amdgcn_raw_buffer_load_b128(rs, wdo + (n & 1) * 112 * 1024, kso(n >> 1), LD_AUX);
    };
    if (w >= 4) {
        const int lw = w - 4, lt = tid - 256;
        u32x4 xr[2], h0[2], h1[2];
        bool wd_early = false;
        auto taps = [&]() {
#pragma unroll
            for (int c2 = 0; c2 < 2; ++c2) {
                const u16* hs = a.ssm + (size_t)(2 * bid + c2) * S;
                h0[c2] = *(const u32x4*)(hs + (lt > 0 ? lt - 1 : 0) * 8);
                h1[c2] = *(const u32x4*)(hs + lt * 8);
            }
        };
        if (lw < 3) {
            const GAS char* gsrc; unsigned vo[2]; size_t cstep, istep; unsigned ldst;
            if (lw < 2) {
                gsrc = (const GAS char*)(lw ? a.wu : a.wg) + (size_t)cb * 256;
                vo[0] = vo[1] = (lane >> 4) * (F * 2) + (((lane & 15) ^ (((lane >> 4) << 2) | (lw << 1))) * 16);
                cstep = (size_t)64 * (F * 2); istep = (size_t)8 * (F * 2);
                ldst = 16384 + lw * 16384;
            } else {
                gsrc = (const GAS char*)a.q + (size_t)rb * 128 * D * 2;
#pragma unroll
                for (int e = 0; e < 2; ++e) { const int row = 8 * e + (lane >> 3); vo[e] = row * (D * 2) + (((lane & 7) ^ ((row >> 1) & 7)) * 16); }
                cstep = 128; istep = (size_t)16 * (D * 2);
                ldst = 0;
            }
            if (lw < 2) vo[1] += 4 * (F * 2);
            const unsigned lds0 = (unsigned)(uintptr_t)smem + ldst;
            const int rot1 = ROT1 ? ((idx >> 2) + 2 * (idx & 3)) % 7 : 0;
            auto pieces = [&](int c, int i0, int i1) {
                const int cc = c + rot1 >= 7 ? c + rot1 - 7 : c + rot1;
#pragma unroll
                for (int i = i0; i < i1; ++i)
                    dma16(gsrc + cc * cstep + (i >> 1) * istep, vo[i & 1], lds0 + (c % 3) * G1_STAGE + i * 1024);
            };
            auto stage = [&](int c) { pieces(c, 0, 16); };
            STAMP(24);
            stage(0);
            STAMP(25);
            pieces(1, 0, PRE);
            STAMP(26);
            asm volatile("s_waitcnt vmcnt(%0)" :: "n"(PRE) : "memory");
            BAR();
            STAMP(1);
            pieces(1, PRE, 16);
#pragma unroll
            for (int c = 0; c < 6; ++c) {
                if (c + 2 <= 6) stage(c + 2);
                asm volatile("" ::: "memory");
                SCHED();
                if (c == 4) taps();
                if (c == 5) {
                    wd_early = __builtin_amdgcn_readfirstlane(*(const LAS unsigned*)(smem + X_HC + 8)) != 0;
                    WAIT_V(4);
                    if (wd_early) {
#pragma unroll
                        for (int n = 0; n < 10; ++n) wdload(n);
                    }
                }
                SCHED();
                STAMP(16 + c);
                if (c == 4) WAIT_V(20);
                else if (c == 5) {}
                else WAIT_V(16);
                BAR();
                STAMP(2 + c);
            }
        } else {
            gu64* mbA = (gu64*)(a.ws + SY_A) + bid;
            const u64 chal = __builtin_amdgcn_s_memrealtime();
            if (lane == 0) __hip_atomic_store((gu64*)(a.ws + SY_M) + bid, chal, RLX);
            u32x4 wt[7], xs[7];
#pragma unroll
            for (int e = 0; e < 7; ++e) {
                const int t = 7 * bid + e, dt = t / 112, ks = t % 112;
                wt[e] = *(const u32x4*)(a.wd + (size_t)(16 * ks + (lane >> 2)) * D + 32 * dt + (lane & 3) * 8);
            }
#pragma unroll
            for (int i = 0; i < 7; ++i) xs[i] = *(const u32x4*)(a.q + (size_t)(8 * bid + (lane >> 3)) * D + ((lane & 7) + 8 * i) * 8);
            if (lane == 0) { *(LAS unsigned*)(smem + X_HC) = 0u; *(LAS unsigned*)(smem + X_HC + 4) = 0u; }
            SCHED();
            BAR();
            STAMP(1);
            u64 ans = __hip_atomic_load(mbA, RLX);
            {
                LAS char* sc = smem + X_SC;
                const int gi = lane >> 4, hf = gi >> 1, blk = gi & 1, q4 = (lane >> 2) & 3, p4 = lane & 3;
                const int ra = (4 * hf + q4) * 64 + (16 * blk + 4 * p4) * 2;
#pragma unroll
                for (int e = 0; e < 7; ++e) *(LAS u32x4*)(sc + e * 1024 + lane * 16) = wt[e];
                asm volatile("s_waitcnt lgkmcnt(0)" ::: "memory");
#pragma unroll
                for (int e = 0; e < 7; ++e) {
                    s16x4 lo = __builtin_amdgcn_ds_read_tr16_b64_v4i16((LAS s16x4*)(sc + e * 1024 + ra));
                    s16x4 hi = __builtin_amdgcn_ds_read_tr16_b64_v4i16((LAS s16x4*)(sc + e * 1024 + ra + 512));
                    s16x8 o = __builtin_shufflevector(lo, hi, 0, 1, 2, 3, 4, 5, 6, 7);
                    __builtin_amdgcn_raw_buffer_store_b128(__builtin_bit_cast(u32x4, o), rs, WS_WDT + (unsigned)(7 * bid + e) * 1024 + lane * 16, 0, SC1);
                }
            }
            STAMP(16);
            BAR();
            STAMP(2);
            if (ans != chal) { if (lane == 0) poll_eq64(mbA, chal); }
            if (lane == 0) (void)__hip_atomic_fetch_add(cnt_ptr(a.ws, C_KV + bid % 7), 1u, RLX);
            STAMP(22);
            {
                const u32x2 o = row_stats(xs);
                if ((lane & 7) == 0) __builtin_amdgcn_raw_buffer_store_b64(o, rs, WS_STATS + (unsigned)(8 * bid + (lane >> 3)) * 8, 0, SC1);
            }
            STAMP(17);
            BAR();
            STAMP(3);
            WAIT_V(0);
            if (lane < 2) (void)__hip_atomic_fetch_add(cnt_ptr(a.ws, lane == 0 ? C_WD + (bid >> 5) : C_ST + (bid >> 4)), 1u, RLX);
            STAMP(23);
            BAR();
            STAMP(4);
            taps();
            BAR();
            STAMP(5);
            const int ci = (lane == 1) ? (rb < 14 ? C_ST + rb : C_HS) : (lane == 2) ? C_WD + ctile : (lane == 3) ? C_CS + (cb >> 1) : C_KV + (bid >> 5);
            const unsigned want = (lane == 1) ? (rb < 14 ? 16u : 4u) : (lane == 3) ? 4u : 32u;
            unsigned cnt = __hip_atomic_load(cnt_ptr(a.ws, ci), RLX);
            wd_early = __builtin_amdgcn_readlane(cnt, 2) >= 32;
            if (lane == 0) *(LAS unsigned*)(smem + X_HC + 8) = wd_early ? 1u : 0u;
            BAR();
            STAMP(6);
            {
                if (!__all(cnt >= want)) cnt = __hip_atomic_load(cnt_ptr(a.ws, ci), RLX);
                if (wd_early) {
#pragma unroll
                    for (int n = 0; n < 10; ++n) wdload(n);
                }
                for (unsigned sp = 0; sp < SPIN_CAP; ++sp) {
                    if (__all(cnt >= want)) break;
                    __builtin_amdgcn_s_sleep(1);
                    cnt = __hip_atomic_load(cnt_ptr(a.ws, ci), RLX);
                }
            }
            STAMP(21);
            BAR();
            STAMP(7);
        }
        const u32x4 cv = __builtin_amdgcn_raw_buffer_load_b128(rs, WS_CS + (unsigned)((lane >> 5) * 1792 + cb * 128 + (lane & 31) * 4) * 4, 0, SC1);
#pragma unroll
        for (int c2 = 0; c2 < 2; ++c2)
            xr[c2] = __builtin_amdgcn_raw_buffer_load_b128(rs, WS_KVR + ((unsigned)(2 * bid + c2) * S + lt * 8) * 2, 0, SC1);
        if (!wd_early) {
#pragma unroll
            for (int n = 0; n < 10; ++n) wdload(n);
        }
#pragma unroll
        for (int n = 10; n < 28; ++n) wdload(n);
        if (lw == 0) *(LAS u32x4*)(smem + X_CS + lane * 16) = cv;
        BAR();
        STAMP(8);
#pragma unroll
        for (int c2 = 0; c2 < 2; ++c2) {
            LAS char* cbp = smem + c2 * CH_BYTES;
            u32x4 hh0 = h0[c2];
            if (lt == 0) hh0 = u32x4{0, 0, 0, 0};
            const unsigned d[8] = {hh0[0], hh0[1], hh0[2], hh0[3], h1[c2][0], h1[c2][1], h1[c2][2], h1[c2][3]};
            unsigned al[7];
#pragma unroll
            for (int i = 0; i < 7; ++i) al[i] = __builtin_amdgcn_alignbit(d[i + 1], d[i], 16);
            const int t = lt + 1;
#pragma unroll
            for (int sg = 0; sg < 8; ++sg) {
                const int sh = sg + 1;
                u32x4 o;
#pragma unroll
                for (int e = 0; e < 4; ++e) o[e] = (sh & 1) ? al[(sh >> 1) + e] : d[(sh >> 1) + e];
                *(LAS u32x4*)(cbp + sg * HC_STRIDE + t * 16) = o;
            }
            if (lt < 8) *(LAS u32x4*)(cbp + lt * HC_STRIDE) = u32x4{0, 0, 0, 0};
            *(LAS u32x4*)(cbp + 8 * HC_STRIDE + lt * 16) = xr[c2];
            if (lt < 32) *(LAS u32x4*)(cbp + 8 * HC_STRIDE + 4096 + lt * 16) = u32x4{0, 0, 0, 0};
        }
        STAMP(9);
    } else {
        const int s0 = (bid / 7) * 64 + (2 * w + half) * 8;
        const int dch = (bid % 7) * 64 + 2 * r;
        unsigned kr[11], vr[8];
#pragma unroll
        for (int i = 0; i < 11; ++i) {
            const int s = s0 - 3 + i;
            asm volatile("global_load_dword %0, %1, %2" : "=v"(kr[i]) : "v"((unsigned)((s < 0 ? 0 : s) * D + dch) * 2u), "s"((const GAS char*)a.k));
        }
#pragma unroll
        for (int i = 0; i < 8; ++i)
            asm volatile("global_load_dword %0, %1, %2" : "=v"(vr[i]) : "v"((unsigned)((s0 + i) * D + dch) * 2u), "s"((const GAS char*)a.v));
        u32x4 sk;
        asm volatile("global_load_dwordx4 %0, %1, %2" : "=v"(sk) : "v"((unsigned)dch * 8u), "s"((const GAS char*)a.sk));
        float ko[8];
        unsigned kpk[4];
        auto kv_part = [&](int c, int hp) {
            const float w0 = bflo(sk[2 * c]), w1 = bfhi(sk[2 * c]), w2 = bflo(sk[2 * c + 1]), w3 = bfhi(sk[2 * c + 1]);
            float kf[7];
#pragma unroll
            for (int i = 0; i < 7; ++i) {
                unsigned kk = kr[4 * hp + i];
                if (hp == 0 && i < 3) kk = (s0 == 0) ? 0u : kk;
                kf[i] = c ? bfhi(kk) : bflo(kk);
            }
#pragma unroll
            for (int i = 0; i < 4; ++i) {
                float acc = kf[i] * w3;
                acc = fmaf(kf[i + 1], w2, acc);
                acc = fmaf(kf[i + 2], w1, acc);
                acc = fmaf(kf[i + 3], w0, acc);
                ko[4 * hp + i] = acc * (c ? bfhi(vr[4 * hp + i]) : bflo(vr[4 * hp + i]));
            }
            kpk[3 - 2 * hp] = pk2(ko[4 * hp + 1], ko[4 * hp + 0]);
            kpk[2 - 2 * hp] = pk2(ko[4 * hp + 3], ko[4 * hp + 2]);
            if (hp == 1)
                __builtin_amdgcn_raw_buffer_store_b128(u32x4{kpk[0], kpk[1], kpk[2], kpk[3]}, rs, WS_KVR + ((unsigned)(dch + c) * S + (2040 - s0)) * 2, 0, SC1);
        };
        const int wm = w >> 1, wn = w & 1;
        unsigned xa[4], wa[4];
        {
            const int gi = lane >> 4, blk = gi & 1, q4 = (lane >> 2) & 3, p4 = lane & 3, gu = p4 >> 1;
#pragma unroll
            for (int t = 0; t < 4; ++t) xa[t] = (unsigned)(wm * 64 + r) * 128 + (((2 * t + half) ^ ((r >> 1) & 7)) * 16);
#pragma unroll
            for (int i = 0; i < 4; ++i) {
                const int pc = 2 * (4 * wn + i) + blk;
                wa[i] = 16384 + gu * 16384 + (8 * half + q4) * 256 + ((pc ^ ((q4 << 2) | (gu << 1))) * 16) + (p4 & 1) * 8;
            }
        }
        f32x16 acc[4][2];
#pragma unroll
        for (int i = 0; i < 4; ++i)
#pragma unroll
            for (int j = 0; j < 2; ++j)
#pragma unroll
                for (int e = 0; e < 16; ++e) acc[i][j][e] = 0.f;
        s16x8 xf[2][2], wf[2][4];
        auto rdfrag = [&](int ks) {
            const int c = ks >> 2, t = ks & 3, b = ks & 1;
            LAS char* sb = smem + (c % 3) * G1_STAGE;
#pragma unroll
            for (int j = 0; j < 2; ++j) xf[b][j] = *(const LAS s16x8*)(sb + xa[t] + j * 4096);
#pragma unroll
            for (int i = 0; i < 4; ++i) {
                s16x4 lo = __builtin_amdgcn_ds_read_tr16_b64_v4i16((LAS s16x4*)(sb + wa[i] + t * 4096));
                s16x4 hi = __builtin_amdgcn_ds_read_tr16_b64_v4i16((LAS s16x4*)(sb + wa[i] + t * 4096 + 1024));
                wf[b][i] = __builtin_shufflevector(lo, hi, 0, 1, 2, 3, 4, 5, 6, 7);
            }
        };
        float mean[2];
        asm volatile("s_waitcnt vmcnt(0)" : "+v"(kr[0]), "+v"(kr[1]), "+v"(kr[2]), "+v"(kr[3]), "+v"(kr[4]), "+v"(kr[5]), "+v"(kr[6]), "+v"(kr[7]), "+v"(kr[8]), "+v"(kr[9]), "+v"(kr[10]),
                     "+v"(vr[0]), "+v"(vr[1]), "+v"(vr[2]), "+v"(vr[3]), "+v"(vr[4]), "+v"(vr[5]), "+v"(vr[6]), "+v"(vr[7]), "+v"(sk));
        STAMP(24);
        kv_part(0, 0); kv_part(0, 1); kv_part(1, 0); kv_part(1, 1);
        STAMP(25);
        BAR();
        STAMP(1);
        unsigned tch[2] = {0u, 0u};
        rdfrag(0);
#pragma unroll
        for (int ks = 0; ks < 28; ++ks) {
            const int c = ks >> 2, t = ks & 3, b = ks & 1;
            if (t == 3 && c < 6) {
                if (c == 0) asm volatile("s_waitcnt vmcnt(0)" : "+v"(tch[0]), "+v"(tch[1]) :: "memory");
                if (c == 5) STAMP(6);
                BAR();
                if (c == 5) {
#pragma unroll
                    for (int j = 0; j < 2; ++j)
                        mean[j] = __uint_as_float(__builtin_amdgcn_raw_buffer_load_b32(rs, WS_STATS + (unsigned)(rb * 128 + (2 * wm + j) * 32 + r) * 8, 0, SC1));
                }
            }
            if (ks < 27) rdfrag(ks + 1);
#pragma unroll
            for (int i = 0; i < 4; ++i) {
                acc[i][0] = mfma32(wf[b][i], xf[b][0], acc[i][0]);
                acc[i][1] = mfma32(wf[b][i], xf[b][1], acc[i][1]);
            }
#pragma unroll
            for (int i = 0; i < 8; ++i) {
                __builtin_amdgcn_sched_group_barrier(0x008, 1, 0);
                if (i < 5) __builtin_amdgcn_sched_group_barrier(0x100, 2, 0);
            }
            SCHED();
        }
        STAMP(7);
        BAR();
        STAMP(8);
        const LAS float* cs = (const LAS float*)(smem + X_CS);
#pragma unroll
        for (int i = 0; i < 4; ++i) {
            const int ct = 4 * wn + i;
            f32x4 cv[4];
#pragma unroll
            for (int g = 0; g < 4; ++g) cv[g] = *(const LAS f32x4*)(cs + (g & 1) * 128 + 16 * ct + 8 * (g >> 1) + 4 * half);
#pragma unroll
            for (int j = 0; j < 2; ++j) {
                float hv[8];
#pragma unroll
                for (int e = 0; e < 4; ++e) {
                    float g0 = acc[i][j][e] - mean[j] * cv[0][e];
                    float u0 = acc[i][j][4 + e] - mean[j] * cv[1][e];
                    float g1 = acc[i][j][8 + e] - mean[j] * cv[2][e];
                    float u1 = acc[i][j][12 + e] - mean[j] * cv[3][e];
                    hv[e] = fmaxf(g0, 0.f) * u0;
                    hv[4 + e] = fmaxf(g1, 0.f) * u1;
                }
                u32x4 o = {pk2(hv[0], hv[1]), pk2(hv[2], hv[3]), pk2(hv[4], hv[5]), pk2(hv[6], hv[7])};
                const int T = rb * 4 + 2 * wm + j, KS = cb * 8 + ct;
                __builtin_amdgcn_raw_buffer_store_b128(o, rs, WS_HT + (((unsigned)T * 112 + KS) * 64 + lane) * 16, 0, SC1);
                wdload(2 * i + j);
                SCHED();
            }
        }
        STAMP(9);
    }
    const float Dval = bflo((unsigned)a.Dv[2 * bid + (w >> 2)]);
    BAR();
    STAMP(10);
    const int cw = w >> 2, qp = w & 3;
    f32x4 cacc[8];
#pragma unroll
    for (int c = 0; c < 8; ++c) cacc[c] = f32x4{0.f, 0.f, 0.f, 0.f};
    {
        LAS char* cbp = smem + cw * CH_BYTES;
        const int m = lane & 15, g = lane >> 4;
        const LAS char* ab0 = cbp + (m & 7) * HC_STRIDE + (8 * qp + g + (m >> 3)) * 16;
        const LAS char* gb0 = cbp + 8 * HC_STRIDE + (2032 - 256 * 7 + 64 * qp - 16 * m + 8 * g) * 2;
        u32x4 A[8], G[8], A2[8], G2[8];
#pragma unroll
        for (int p = 0; p < 8; ++p) {
            G[p] = *(const LAS u32x4*)(gb0 + (7 - p) * 512);
            A[p] = *(const LAS u32x4*)(ab0 + p * 512);
        }
        SCHED();
#pragma unroll
        for (int p = 0; p < 8; ++p) {
#pragma unroll
            for (int c = p; c < 8; ++c) cacc[c] = mfma16u(A[p], G[c - p], cacc[c]);
            A2[p] = *(const LAS u32x4*)(ab0 + 64 + p * 512);
            G2[7 - p] = *(const LAS u32x4*)(gb0 + 64 + p * 512);
            if (w < 4) { wdload(8 + 2 * p); wdload(9 + 2 * p); if (p < 4) wdload(24 + p); }
            if (p == 3 && w < 4) {
                WAIT_V(13);
                unsigned old = 0u;
                if (lane == 0) old = __hip_atomic_fetch_add((LAS unsigned*)(smem + X_HC), 1u, __ATOMIC_RELAXED, __HIP_MEMORY_SCOPE_WORKGROUP);
                if (lane == 0 && old == 3u) (void)__hip_atomic_fetch_add(cnt_ptr(a.ws, C_H + rb), 1u, RLX);
            }
            SCHED();
        }
#pragma unroll
        for (int mm = 7; mm >= 0; --mm) {
#pragma unroll
            for (int p = 0; p + mm < 8; ++p) cacc[p + mm] = mfma16u(A2[p], G2[mm], cacc[p + mm]);
        }
    }
    f32x4 keep[2];
    {
        LAS f32x4* red = (LAS f32x4*)(smem + X_CRED) + w * 512;
#pragma unroll
        for (int j = 0; j < 4; ++j) {
            if (qp != j) { red[(2 * j) * 64 + lane] = cacc[2 * j]; red[(2 * j + 1) * 64 + lane] = cacc[2 * j + 1]; }
            else { keep[0] = cacc[2 * j]; keep[1] = cacc[2 * j + 1]; }
        }
    }
    BAR();
    STAMP(11);
    {
        const LAS f32x4* red = (const LAS f32x4*)(smem + X_CRED) + cw * 2048;
        const int ap = lane & 15, g = lane >> 4;
        const LAS char* xrl = smem + cw * CH_BYTES + 8 * HC_STRIDE;
#pragma unroll
        for (int e2 = 0; e2 < 2; ++e2) {
            const int c = 2 * qp + e2;
            f32x4 s = keep[e2];
#pragma unroll
            for (int j = 1; j < 4; ++j) s += red[((qp + j) & 3) * 512 + c * 64 + lane];
            const int s0 = 256 * c + 16 * ap + 4 * g;
            const u32x2 kx = *(const LAS u32x2*)(xrl + (2044 - s0) * 2);
            const float kv0 = bfhi(kx[1]), kv1 = bflo(kx[1]), kv2 = bfhi(kx[0]), kv3 = bflo(kx[0]);
            const float y0 = rbf(s[0]) + rbf(kv0 * Dval), y1 = rbf(s[1]) + rbf(kv1 * Dval);
            const float y2 = rbf(s[2]) + rbf(kv2 * Dval), y3 = rbf(s[3]) + rbf(kv3 * Dval);
            u32x2 o = {pk2(y0, y1), pk2(y2, y3)};
            __builtin_amdgcn_raw_buffer_store_b64(o, rs, WS_YT + ((unsigned)(2 * bid + cw) * S + s0) * 2, 0, SC1);
        }
    }
    if (lane == 0) poll_ge(cnt_ptr(a.ws, C_H + (rt >> 1)), 14u);
    WAIT_V(0);
    {
        unsigned old = 0u;
        if (lane == 0) old = __hip_atomic_fetch_add((LAS unsigned*)(smem + X_HC + 4), 1u, __ATOMIC_RELAXED, __HIP_MEMORY_SCOPE_WORKGROUP);
        if (lane == 0 && old == 7u) (void)__hip_atomic_fetch_add(cnt_ptr(a.ws, C_Y + ((2 * bid) >> 6)), 1u, RLX);
    }
    STAMP(12);
    u32x4 hw[HWIN][2];
    auto hload = [&](int i) {
        hw[i % HWIN][0] = __builtin_amdgcn_raw_buffer_load_b128(rs, hto, kso(i), LD_AUX);
        hw[i % HWIN][1] = __builtin_amdgcn_raw_buffer_load_b128(rs, hto + 112 * 1024, kso(i), LD_AUX);
    };
#pragma unroll
    for (int i = 0; i < HWIN; ++i) hload(i);
    const int tp = tid >> 4, cq = tid & 15;
    const int tok0 = rt * 64 + 2 * tp, d0 = ctile * 64 + 4 * cq;
    u32x2 q0, q1; u32x4 stu;
    SCHED();
    BAR();
    f32x16 acc2[2][2];
#pragma unroll
    for (int i = 0; i < 2; ++i)
#pragma unroll
        for (int j = 0; j < 2; ++j)
#pragma unroll
            for (int e = 0; e < 16; ++e) acc2[i][j][e] = 0.f;
    unsigned yv[4];
#pragma unroll
    for (int i = 0; i < 14; ++i) {
        const u32x4 hf0 = hw[i % HWIN][0], hf1 = hw[i % HWIN][1];
        if (i + HWIN < 14) hload(i + HWIN);
        if (i == 9) {
            if (lane == 0) poll_ge(cnt_ptr(a.ws, C_Y + ctile), 32u);
            __builtin_amdgcn_fence(__ATOMIC_ACQUIRE, "wavefront");
#pragma unroll
            for (int j = 0; j < 4; ++j)
                yv[j] = __builtin_amdgcn_raw_buffer_load_b32(rs, WS_YT + (unsigned)(d0 + j) * 4096 + tok0 * 2, 0, SC1);
            q0 = *(const u32x2*)(a.q + (size_t)tok0 * D + d0);
            q1 = *(const u32x2*)(a.q + (size_t)(tok0 + 1) * D + d0);
            stu = __builtin_amdgcn_raw_buffer_load_b128(rs, WS_STATS + (unsigned)tok0 * 8, 0, SC1);
        }
        const u32x4 wf0 = wd[i % WWIN][0], wf1 = wd[i % WWIN][1];
        acc2[0][0] = mfma32u(wf0, hf0, acc2[0][0]);
        acc2[0][1] = mfma32u(wf0, hf1, acc2[0][1]);
        acc2[1][0] = mfma32u(wf1, hf0, acc2[1][0]);
        acc2[1][1] = mfma32u(wf1, hf1, acc2[1][1]);
        SCHED();
    }
    STAMP(13);
    {
        LAS char* rbp = smem + w * (64 * RED_STRIDE);
#pragma unroll
        for (int i = 0; i < 2; ++i)
#pragma unroll
            for (int j = 0; j < 2; ++j)
#pragma unroll
                for (int g = 0; g < 4; ++g) {
                    f32x4 v = {acc2[i][j][4 * g], acc2[i][j][4 * g + 1], acc2[i][j][4 * g + 2], acc2[i][j][4 * g + 3]};
                    *(LAS f32x4*)(rbp + (j * 32 + r) * RED_STRIDE + (i * 32 + 8 * g + 4 * half) * 4) = v;
                }
    }
    float xv[2][4], sv[2][4], r2[2];
#pragma unroll
    for (int e = 0; e < 2; ++e) {
        const float mb = __uint_as_float(stu[2 * e]), rd = __uint_as_float(stu[2 * e + 1]);
        const u32x2 qq = e ? q1 : q0;
        r2[e] = rd * rd;
#pragma unroll
        for (int j = 0; j < 4; ++j) {
            const float x = (j & 1) ? bfhi(qq[j >> 1]) : bflo(qq[j >> 1]);
            const float xn = rbf(rbf(x - mb) * rd);
            const float y = e ? bfhi(yv[j]) : bflo(yv[j]);
            xv[e][j] = x; sv[e][j] = rbf(xn * y);
            asm volatile("" : "+v"(xv[e][j]), "+v"(sv[e][j]));
        }
    }
    BAR();
    {
        f32x4 o[2];
#pragma unroll
        for (int e = 0; e < 2; ++e) {
            const LAS char* rbp = smem + (2 * tp + e) * RED_STRIDE + cq * 16;
            f32x4 s0 = *(const LAS f32x4*)rbp, s1 = *(const LAS f32x4*)(rbp + 64 * RED_STRIDE);
#pragma unroll
            for (int ww = 2; ww < 8; ww += 2) { s0 += *(const LAS f32x4*)(rbp + ww * (64 * RED_STRIDE)); s1 += *(const LAS f32x4*)(rbp + (ww + 1) * (64 * RED_STRIDE)); }
            o[e] = s0 + s1;
        }
#pragma unroll
        for (int e = 0; e < 2; ++e) {
            float res[4];
#pragma unroll
            for (int j = 0; j < 4; ++j) {
                const float ov = rbf(o[e][j] * r2[e]);
                const float gg = rbf(ov * __builtin_amdgcn_rcpf(1.0f + __expf(-ov)));
                res[j] = xv[e][j] + rbf(sv[e][j] * gg);
            }
            u32x2 ov2 = {pk2(res[0], res[1]), pk2(res[2], res[3])};
            __builtin_amdgcn_raw_buffer_store_b64(ov2, mkrs(a.out, S * D * 2), (unsigned)((tok0 + e) * D + d0) * 2u, 0, SC1);
        }
    }
    STAMP(15);
    STAMP_FLUSH();
}

extern "C" void kernel_launch(void* const* d_in, const int* in_sizes, int n_in,
                              void* d_out, int out_size, void* d_ws, size_t ws_size,
                              hipStream_t stream) {
    (void)hipFuncSetAttribute((const void*)fused_kernel, hipFuncAttributeMaxDynamicSharedMemorySize, LDS_BYTES);
    Args a;
    a.q = (const u16*)d_in[0]; a.k = (const u16*)d_in[1]; a.v = (const u16*)d_in[2]; a.sk = (const u16*)d_in[3];
    a.ssm = (const u16*)d_in[4]; a.Dv = (const u16*)d_in[5];
    a.wg = (const u16*)d_in[7]; a.wu = (const u16*)d_in[8]; a.wd = (const u16*)d_in[9];
    a.ws = (char*)d_ws; a.out = (u16*)d_out;
    fused_kernel<<<NMAIN + NPREP + 1, 512, LDS_BYTES, stream>>>(a);
}
```

```cpp
#include <hip/hip_runtime.h>
#include <stdint.h>

typedef unsigned short u16;
typedef __attribute__((ext_vector_type(4))) short s16x4;
typedef __attribute__((ext_vector_type(8))) short s16x8;
typedef __attribute__((ext_vector_type(8))) __bf16 bf16x8;
typedef __attribute__((ext_vector_type(2))) __bf16 bf16x2;
typedef __attribute__((ext_vector_type(16))) float f32x16;
typedef __attribute__((ext_vector_type(4))) float f32x4;
typedef __attribute__((ext_vector_type(4))) unsigned u32x4;
typedef __attribute__((ext_vector_type(2))) unsigned u32x2;
#define GAS __attribute__((address_space(1)))
#define LAS __attribute__((address_space(3)))
#define DI __device__ __forceinline__
typedef GAS unsigned gu32;
typedef GAS unsigned long long gu64;
typedef unsigned long long u64;
#define RLX __ATOMIC_RELAXED, __HIP_MEMORY_SCOPE_AGENT

static constexpr int S = 2048, D = 448, F = 1792;
static constexpr int NMAIN = 224, NPREP = 28;
#define STAMP_DECL
#define STAMP(i)
#define STAMP_FLUSH()

static constexpr unsigned SY_M = 0, SY_A = 2048, SY_CNT = 4096;
static constexpr int C_KV = 0, C_ST = 7, C_HS = 21, C_WD = 22, C_H = 29, C_Y = 45, C_CS = 52, C_N = 59;
static constexpr unsigned WS_STATS = 16384;
static constexpr unsigned WS_CS    = WS_STATS + S * 8;
static constexpr unsigned WS_HT    = WS_CS + 16384;
static constexpr unsigned WS_WDT   = WS_HT + S * F * 2;
static constexpr unsigned WS_KVR   = WS_WDT + F * D * 2;
static constexpr unsigned WS_YT    = WS_KVR + S * D * 2;
static constexpr unsigned WS_END   = WS_YT + S * D * 2;
static constexpr int SC1 = 16;
#define DRY 0
#define ROT1 0
#define PAGE_TOUCH 0
#define CODE_LINES 0
#define PRE 8
#define HDELAY 3
#define TCH0 7
#define LD_AUX 16
static constexpr unsigned SPIN_CAP = 1u << 16;

DI float bflo(unsigned u) { return __uint_as_float(u << 16); }
DI float bfhi(unsigned u) { return __uint_as_float(u & 0xffff0000u); }
DI unsigned pk2(float a, float b) { bf16x2 r = {(__bf16)a, (__bf16)b}; return __builtin_bit_cast(unsigned, r); }
DI float rbf(float a) { return bflo(pk2(a, a)); }
template <int CTRL> DI float dpp_add(float v) {
    return v + __int_as_float(__builtin_amdgcn_update_dpp(0, __float_as_int(v), CTRL, 0xf, 0xf, true));
}
DI float sum8(float v) { v = dpp_add<0xB1>(v); v = dpp_add<0x4E>(v); return dpp_add<0x141>(v); }
DI float xor16_add(float v) { auto r = __builtin_amdgcn_permlane16_swap(__float_as_uint(v), __float_as_uint(v), false, false); return __uint_as_float(r[0]) + __uint_as_float(r[1]); }
DI float xor32_add(float v) { auto r = __builtin_amdgcn_permlane32_swap(__float_as_uint(v), __float_as_uint(v), false, false); return __uint_as_float(r[0]) + __uint_as_float(r[1]); }
DI float sum32(float v) { v = sum8(v); v = dpp_add<0x140>(v); return xor16_add(v); }
DI f32x16 mfma32(s16x8 a, s16x8 b, f32x16 c) {
    return __builtin_amdgcn_mfma_f32_32x32x16_bf16(__builtin_bit_cast(bf16x8, a), __builtin_bit_cast(bf16x8, b), c, 0, 0, 0);
}
DI f32x4 mfma16u(u32x4 a, u32x4 b, f32x4 c) {
    return __builtin_amdgcn_mfma_f32_16x16x32_bf16(__builtin_bit_cast(bf16x8, a), __builtin_bit_cast(bf16x8, b), c, 0, 0, 0);
}
DI f32x16 mfma32u(u32x4 a, u32x4 b, f32x16 c) { return mfma32(__builtin_bit_cast(s16x8, a), __builtin_bit_cast(s16x8, b), c); }
#define WAIT_V(n) asm volatile("s_waitcnt vmcnt(" #n ")" ::: "memory")
#define BAR() do { asm volatile("s_waitcnt lgkmcnt(0)" ::: "memory"); __builtin_amdgcn_s_barrier(); asm volatile("" ::: "memory"); } while (0)
#define SCHED() __builtin_amdgcn_sched_barrier(0)
DI __amdgpu_buffer_rsrc_t mkrs(const void* p, unsigned bytes) { return __builtin_amdgcn_make_buffer_rsrc((void*)p, 0, bytes, 0x00020000); }
DI gu32* cnt_ptr(char* ws, int i) { return (gu32*)(ws + SY_CNT + 128 * i); }
DI void poll_eq(gu32* p, unsigned want) {
    for (unsigned sp = 0; sp < SPIN_CAP; ++sp) { if (__hip_atomic_load(p, RLX) == want) break; __builtin_amdgcn_s_sleep(1); }
}
DI void poll_eq64(gu64* p, u64 want) {
    for (unsigned sp = 0; sp < SPIN_CAP; ++sp) { if (__hip_atomic_load(p, RLX) == want) break; __builtin_amdgcn_s_sleep(1); }
}
DI void poll_ge(gu32* p, unsigned want) {
    for (unsigned sp = 0; sp < SPIN_CAP; ++sp) { if (__hip_atomic_load(p, RLX) >= want) break; __builtin_amdgcn_s_sleep(1); }
}

struct Args {
    const u16* q; const u16* k; const u16* v; const u16* sk; const u16* ssm; const u16* Dv;
    const u16* wg; const u16* wu; const u16* wd;
    char* ws; u16* out;
};

static constexpr int G1_STAGE = 49152;
static constexpr int X_CS = 3 * G1_STAGE;
static constexpr int HC_STRIDE = 4160;
static constexpr int CH_BYTES = 8 * HC_STRIDE + 4608;
static constexpr int X_CRED = 2 * CH_BYTES;
static constexpr int RED_STRIDE = 272;
static constexpr int X_HC = X_CS + 1024;
static constexpr int X_SC = X_HC + 16;
static constexpr int LDS_BYTES = X_SC + 8192;
static constexpr int WWIN = 14;
static constexpr int HWIN = 6;

DI void dma16(const GAS char* sbase, unsigned voff, unsigned ldst) {
    unsigned keep;
    asm volatile("s_mov_b32 %0, m0\n\ts_mov_b32 m0, %3\n\ts_nop 0\n\tglobal_load_lds_dwordx4 %1, %2\n\ts_mov_b32 m0, %0"
                 : "=&s"(keep) : "v"(voff), "s"(sbase), "s"(ldst));
}

DI void service_block(const Args& a, LAS char* smem) {
    const int lane = threadIdx.x & 63, w = threadIdx.x >> 6;
    const __amdgpu_buffer_rsrc_t rs = mkrs(a.ws, WS_END);
    volatile LAS unsigned* stop = (volatile LAS unsigned*)smem;
    if (w == 0) {
        if (lane < C_N) __hip_atomic_store(cnt_ptr(a.ws, lane), 0u, RLX);
        if (lane == 0) *stop = 0u;
        WAIT_V(0);
    }
    BAR();
    if (w == 0) {
        for (unsigned it = 0; it < SPIN_CAP; ++it) {
            const unsigned c = __hip_atomic_load(cnt_ptr(a.ws, lane < C_N ? lane : 0), RLX);
            const bool ok = (lane >= C_CS && lane < C_N) ? (c >= 4u) : (lane < C_ST) ? (c >= 32u) : true;
            if (__all(ok)) break;
            __builtin_amdgcn_s_sleep(8);
        }
        if (lane == 0) *stop = 1u;
    } else if (w < 3) {
        for (int i = 0; i < (w - 1) * 4; ++i) __builtin_amdgcn_s_sleep(2);
        for (unsigned it = 0; it < SPIN_CAP; ++it) {
            const u32x4 m0 = __builtin_amdgcn_raw_buffer_load_b128(rs, SY_M + lane * 32, 0, SC1);
            const u32x4 m1 = __builtin_amdgcn_raw_buffer_load_b128(rs, SY_M + lane * 32 + 16, 0, SC1);
            __builtin_amdgcn_raw_buffer_store_b128(m0, rs, SY_A + lane * 32, 0, SC1);
            __builtin_amdgcn_raw_buffer_store_b128(m1, rs, SY_A + lane * 32 + 16, 0, SC1);
            if (*stop) break;
            __builtin_amdgcn_s_sleep(6);
        }
    }
}

DI u32x2 row_stats(const u32x4 (&xs)[7]) {
    float s0 = 0.f, s1 = 0.f;
#pragma unroll
    for (int i = 0; i < 7; ++i)
#pragma unroll
        for (int j = 0; j < 4; ++j) { s0 += bflo(xs[i][j]); s1 += bfhi(xs[i][j]); }
    float sum = s0 + s1;
    sum = sum8(sum);
    const float m = sum * (1.0f / D);
    float q0 = 0.f, q1 = 0.f;
#pragma unroll
    for (int i = 0; i < 7; ++i)
#pragma unroll
        for (int j = 0; j < 4; ++j) {
            const float d0 = bflo(xs[i][j]) - m, d1 = bfhi(xs[i][j]) - m;
            q0 = fmaf(d0, d0, q0); q1 = fmaf(d1, d1, q1);
        }
    float sq = q0 + q1;
    sq = sum8(sq);
    const float var = sq * (1.0f / D);
    const float mb = rbf(m), vb = rbf(var);
    const float den = rbf(sqrtf(rbf(vb + 1e-5f)));
    return u32x2{__float_as_uint(mb), __float_as_uint(1.0f / den)};
}

#define PSTAMP(i)
DI void prep_block(const Args& a, LAS char* smem) {
    const int tid = threadIdx.x, lane = tid & 63;
    const int w = __builtin_amdgcn_readfirstlane(tid >> 6);
    const int g = (blockIdx.x - NMAIN) * 8 + w;
    const __amdgpu_buffer_rsrc_t rs = mkrs(a.ws, WS_END);
    gu64* mbA = (gu64*)(a.ws + SY_A) + blockIdx.x;
    const u64 chal = __builtin_amdgcn_s_memrealtime();
    if (tid == 0) __hip_atomic_store((gu64*)(a.ws + SY_M) + blockIdx.x, chal, RLX);
    for (int i = 0; i < HDELAY; ++i) __builtin_amdgcn_s_sleep(16);
    const int hb = blockIdx.x - NMAIN;
    const u16* wsrc = (hb < 14 ? a.wg : a.wu) + (hb < 14 ? hb : hb - 14) * 128 + (tid & 15) * 8;
    u32x4 wr[14];
#pragma unroll
    for (int i = 0; i < 14; ++i) wr[i] = *(const u32x4*)(wsrc + (size_t)((tid >> 4) * 14 + i) * F);
    u32x4 xs[7];
    if (g < 32) {
        const int srow = (224 + g) * 8 + (lane >> 3);
#pragma unroll
        for (int i = 0; i < 7; ++i) xs[i] = *(const u32x4*)(a.q + (size_t)srow * D + ((lane & 7) + 8 * i) * 8);
    }
    SCHED();
    PSTAMP(1);
    float cs[8];
#pragma unroll
    for (int j = 0; j < 8; ++j) cs[j] = 0.f;
#pragma unroll
    for (int i = 0; i < 14; ++i)
#pragma unroll
        for (int e = 0; e < 4; ++e) { cs[2 * e] += bflo(wr[i][e]); cs[2 * e + 1] += bfhi(wr[i][e]); }
#pragma unroll
    for (int j = 0; j < 8; ++j) cs[j] = xor32_add(xor16_add(cs[j]));
    LAS f32x4* red = (LAS f32x4*)smem;
    if (lane < 16) { red[(w * 16 + lane) * 2] = f32x4{cs[0], cs[1], cs[2], cs[3]}; red[(w * 16 + lane) * 2 + 1] = f32x4{cs[4], cs[5], cs[6], cs[7]}; }
    if (g < 32) {
        const u32x2 o = row_stats(xs);
        if ((lane & 7) == 0) __builtin_amdgcn_raw_buffer_store_b64(o, rs, WS_STATS + (unsigned)((224 + g) * 8 + (lane >> 3)) * 8, 0, SC1);
    }
    BAR();
    if (tid < 32) {
        f32x4 s = red[tid];
#pragma unroll
        for (int ww = 1; ww < 8; ++ww) s += red[ww * 32 + tid];
        __builtin_amdgcn_raw_buffer_store_b128(__builtin_bit_cast(u32x4, s), rs, WS_CS + (unsigned)((hb < 14 ? 0 : 1792) + (hb < 14 ? hb : hb - 14) * 128 + tid * 4) * 4, 0, SC1);
    }
    PSTAMP(4);
    WAIT_V(0);
    PSTAMP(5);
    BAR();
    PSTAMP(6);
    if (w == 0) {
        if (lane == 0) poll_eq64(mbA, chal);
        const int cbh = hb < 14 ? hb : hb - 14;
        if (lane == 0 || (lane == 1 && hb < 4)) (void)__hip_atomic_fetch_add(cnt_ptr(a.ws, lane == 0 ? C_CS + (cbh >> 1) : C_HS), 1u, RLX);
    }
}

__global__ __launch_bounds__(512, 2) void fused_kernel(Args a) {
    extern __shared__ __attribute__((aligned(16))) char smem_[];
    LAS char* smem = (LAS char*)smem_;
    const int bid = blockIdx.x;
    u64 kernel_pc;
    asm volatile("s_getpc_b64 %0" : "=s"(kernel_pc));
    STAMP_DECL
    STAMP(0);
    if (bid >= NMAIN) {
        if (bid == NMAIN + NPREP) service_block(a, smem); else prep_block(a, smem);
        STAMP(15); STAMP_FLUSH();
        return;
    }
    const int tid = threadIdx.x, lane = tid & 63;
    const int w = __builtin_amdgcn_readfirstlane(tid >> 6);
    const int xcd = bid & 7, idx = bid >> 3;
    const __amdgpu_buffer_rsrc_t rs = mkrs(a.ws, WS_END);
    const int rb = (xcd >> 1) * 4 + (idx & 3);
    const int cb = (xcd & 1) * 7 + (idx >> 2);
    const int rt = xcd * 4 + (idx & 3);
    const int ctile = idx >> 2;
    const int r = lane & 31, half = lane >> 5;
    const int rot = (2 * ctile + 3 * (idx & 3) + (idx & 2) / 2) % 14;
    const unsigned wdo = WS_WDT + (((unsigned)(ctile * 2) * 112 + 14 * w) * 64 + lane) * 16;
    const unsigned hto = WS_HT + (((unsigned)(rt * 2) * 112 + 14 * w) * 64 + lane) * 16;
    auto kso = [&](int i) { const int k = i + rot; return (unsigned)(k >= 14 ? k - 14 : k) * 1024u; };
    u32x4 wd[WWIN][2];
    auto wdload = [&](int n) {
        wd[(n >> 1) % WWIN][n & 1] = __builtin_amdgcn_raw_buffer_load_b128(rs, wdo + (n & 1) * 112 * 1024, kso(n >> 1), LD_AUX);
    };
    if (w >= 4) {
        const int lw = w - 4, lt = tid - 256;
        u32x4 xr[2], h0[2], h1[2];
        bool wd_early = false;
        auto taps = [&]() {
#pragma unroll
            for (int c2 = 0; c2 < 2; ++c2) {
                const u16* hs = a.ssm + (size_t)(2 * bid + c2) * S;
                h0[c2] = *(const u32x4*)(hs + (lt > 0 ? lt - 1 : 0) * 8);
                h1[c2] = *(const u32x4*)(hs + lt * 8);
            }
        };
        if (lw < 3) {
            const GAS char* gsrc; unsigned vo[2]; size_t cstep, istep; unsigned ldst;
            if (lw < 2) {
                gsrc = (const GAS char*)(lw ? a.wu : a.wg) + (size_t)cb * 256;
                vo[0] = vo[1] = (lane >> 4) * (F * 2) + (((lane & 15) ^ (((lane >> 4) << 2) | (lw << 1))) * 16);
                cstep = (size_t)64 * (F * 2); istep = (size_t)8 * (F * 2);
                ldst = 16384 + lw * 16384;
            } else {
                gsrc = (const GAS char*)a.q + (size_t)rb * 128 * D * 2;
#pragma unroll
                for (int e = 0; e < 2; ++e) { const int row = 8 * e + (lane >> 3); vo[e] = row * (D * 2) + (((lane & 7) ^ ((row >> 1) & 7)) * 16); }
                cstep = 128; istep = (size_t)16 * (D * 2);
                ldst = 0;
            }
            if (lw < 2) vo[1] += 4 * (F * 2);
            const unsigned lds0 = (unsigned)(uintptr_t)smem + ldst;
            const int rot1 = ROT1 ? ((idx >> 2) + 2 * (idx & 3)) % 7 : 0;
            auto pieces = [&](int c, int i0, int i1) {
                const int cc = c + rot1 >= 7 ? c + rot1 - 7 : c + rot1;
#pragma unroll
                for (int i = i0; i < i1; ++i)
                    dma16(gsrc + cc * cstep + (i >> 1) * istep, vo[i & 1], lds0 + (c % 3) * G1_STAGE + i * 1024);
            };
            auto stage = [&](int c) { pieces(c, 0, 16); };
            STAMP(24);
            stage(0);
            STAMP(25);
            pieces(1, 0, PRE);
            STAMP(26);
            asm volatile("s_waitcnt vmcnt(%0)" :: "n"(PRE) : "memory");
            BAR();
            STAMP(1);
            pieces(1, PRE, 16);
#pragma unroll
            for (int c = 0; c < 6; ++c) {
                if (c + 2 <= 6) stage(c + 2);
                asm volatile("" ::: "memory");
                SCHED();
                if (c == 4) taps();
                if (c == 5) {
                    wd_early = __builtin_amdgcn_readfirstlane(*(const LAS unsigned*)(smem + X_HC + 8)) != 0;
                    WAIT_V(4);
                    if (wd_early) {
#pragma unroll
                        for (int n = 0; n < 10; ++n) wdload(n);
                    }
                }
                SCHED();
                STAMP(16 + c);
                if (c == 4) WAIT_V(20);
                else if (c == 5) {}
                else WAIT_V(16);
                BAR();
                STAMP(2 + c);
            }
        } else {
            gu64* mbA = (gu64*)(a.ws + SY_A) + bid;
            const u64 chal = __builtin_amdgcn_s_memrealtime();
            if (lane == 0) __hip_atomic_store((gu64*)(a.ws + SY_M) + bid, chal, RLX);
            u32x4 wt[7], xs[7];
#pragma unroll
            for (int e = 0; e < 7; ++e) {
                const int t = 7 * bid + e, dt = t / 112, ks = t % 112;
                wt[e] = *(const u32x4*)(a.wd + (size_t)(16 * ks + (lane >> 2)) * D + 32 * dt + (lane & 3) * 8);
            }
#pragma unroll
            for (int i = 0; i < 7; ++i) xs[i] = *(const u32x4*)(a.q + (size_t)(8 * bid + (lane >> 3)) * D + ((lane & 7) + 8 * i) * 8);
            if (lane == 0) { *(LAS unsigned*)(smem + X_HC) = 0u; *(LAS unsigned*)(smem + X_HC + 4) = 0u; }
            SCHED();
            BAR();
            STAMP(1);
            u64 ans = __hip_atomic_load(mbA, RLX);
            {
                LAS char* sc = smem + X_SC;
                const int gi = lane >> 4, hf = gi >> 1, blk = gi & 1, q4 = (lane >> 2) & 3, p4 = lane & 3;
                const int ra = (4 * hf + q4) * 64 + (16 * blk + 4 * p4) * 2;
#pragma unroll
                for (int e = 0; e < 7; ++e) *(LAS u32x4*)(sc + e * 1024 + lane * 16) = wt[e];
                asm volatile("s_waitcnt lgkmcnt(0)" ::: "memory");
#pragma unroll
                for (int e = 0; e < 7; ++e) {
                    s16x4 lo = __builtin_amdgcn_ds_read_tr16_b64_v4i16((LAS s16x4*)(sc + e * 1024 + ra));
                    s16x4 hi = __builtin_amdgcn_ds_read_tr16_b64_v4i16((LAS s16x4*)(sc + e * 1024 + ra + 512));
                    s16x8 o = __builtin_shufflevector(lo, hi, 0, 1, 2, 3, 4, 5, 6, 7);
                    __builtin_amdgcn_raw_buffer_store_b128(__builtin_bit_cast(u32x4, o), rs, WS_WDT + (unsigned)(7 * bid + e) * 1024 + lane * 16, 0, SC1);
                }
            }
            STAMP(16);
            BAR();
            STAMP(2);
            if (ans != chal) { if (lane == 0) poll_eq64(mbA, chal); }
            if (lane == 0) (void)__hip_atomic_fetch_add(cnt_ptr(a.ws, C_KV + bid % 7), 1u, RLX);
            STAMP(22);
            {
                const u32x2 o = row_stats(xs);
                if ((lane & 7) == 0) __builtin_amdgcn_raw_buffer_store_b64(o, rs, WS_STATS + (unsigned)(8 * bid + (lane >> 3)) * 8, 0, SC1);
            }
            STAMP(17);
            BAR();
            STAMP(3);
            WAIT_V(0);
            if (lane < 2) (void)__hip_atomic_fetch_add(cnt_ptr(a.ws, lane == 0 ? C_WD + (bid >> 5) : C_ST + (bid >> 4)), 1u, RLX);
            STAMP(23);
            BAR();
            STAMP(4);
            taps();
            BAR();
            STAMP(5);
            const int ci = (lane == 1) ? (rb < 14 ? C_ST + rb : C_HS) : (lane == 2) ? C_WD + ctile : (lane == 3) ? C_CS + (cb >> 1) : C_KV + (bid >> 5);
            const unsigned want = (lane == 1) ? (rb < 14 ? 16u : 4u) : (lane == 3) ? 4u : 32u;
            unsigned cnt = __hip_atomic_load(cnt_ptr(a.ws, ci), RLX);
            wd_early = __builtin_amdgcn_readlane(cnt, 2) >= 32;
            if (lane == 0) *(LAS unsigned*)(smem + X_HC + 8) = wd_early ? 1u : 0u;
            BAR();
            STAMP(6);
            {
                if (!__all(cnt >= want)) cnt = __hip_atomic_load(cnt_ptr(a.ws, ci), RLX);
                if (wd_early) {
#pragma unroll
                    for (int n = 0; n < 10; ++n) wdload(n);
                }
                for (unsigned sp = 0; sp < SPIN_CAP; ++sp) {
                    if (__all(cnt >= want)) break;
                    __builtin_amdgcn_s_sleep(1);
                    cnt = __hip_atomic_load(cnt_ptr(a.ws, ci), RLX);
                }
            }
            STAMP(21);
            BAR();
            STAMP(7);
        }
        const u32x4 cv = __builtin_amdgcn_raw_buffer_load_b128(rs, WS_CS + (unsigned)((lane >> 5) * 1792 + cb * 128 + (lane & 31) * 4) * 4, 0, SC1);
#pragma unroll
        for (int c2 = 0; c2 < 2; ++c2)
            xr[c2] = __builtin_amdgcn_raw_buffer_load_b128(rs, WS_KVR + ((unsigned)(2 * bid + c2) * S + lt * 8) * 2, 0, SC1);
        if (!wd_early) {
#pragma unroll
            for (int n = 0; n < 10; ++n) wdload(n);
        }
#pragma unroll
        for (int n = 10; n < 28; ++n) wdload(n);
        if (lw == 0) *(LAS u32x4*)(smem + X_CS + lane * 16) = cv;
        BAR();
        STAMP(8);
#pragma unroll
        for (int c2 = 0; c2 < 2; ++c2) {
            LAS char* cbp = smem + c2 * CH_BYTES;
            u32x4 hh0 = h0[c2];
            if (lt == 0) hh0 = u32x4{0, 0, 0, 0};
            const unsigned d[8] = {hh0[0], hh0[1], hh0[2], hh0[3], h1[c2][0], h1[c2][1], h1[c2][2], h1[c2][3]};
            unsigned al[7];
#pragma unroll
            for (int i = 0; i < 7; ++i) al[i] = __builtin_amdgcn_alignbit(d[i + 1], d[i], 16);
            const int t = lt + 1;
#pragma unroll
            for (int sg = 0; sg < 8; ++sg) {
                const int sh = sg + 1;
                u32x4 o;
#pragma unroll
                for (int e = 0; e < 4; ++e) o[e] = (sh & 1) ? al[(sh >> 1) + e] : d[(sh >> 1) + e];
                *(LAS u32x4*)(cbp + sg * HC_STRIDE + t * 16) = o;
            }
            if (lt < 8) *(LAS u32x4*)(cbp + lt * HC_STRIDE) = u32x4{0, 0, 0, 0};
            *(LAS u32x4*)(cbp + 8 * HC_STRIDE + lt * 16) = xr[c2];
            if (lt < 32) *(LAS u32x4*)(cbp + 8 * HC_STRIDE + 4096 + lt * 16) = u32x4{0, 0, 0, 0};
        }
        STAMP(9);
    } else {
        const int s0 = (bid / 7) * 64 + (2 * w + half) * 8;
        const int dch = (bid % 7) * 64 + 2 * r;
        unsigned kr[11], vr[8];
#pragma unroll
        for (int i = 0; i < 11; ++i) {
            const int s = s0 - 3 + i;
            asm volatile("global_load_dword %0, %1, %2" : "=v"(kr[i]) : "v"((unsigned)((s < 0 ? 0 : s) * D + dch) * 2u), "s"((const GAS char*)a.k));
        }
#pragma unroll
        for (int i = 0; i < 8; ++i)
            asm volatile("global_load_dword %0, %1, %2" : "=v"(vr[i]) : "v"((unsigned)((s0 + i) * D + dch) * 2u), "s"((const GAS char*)a.v));
        u32x4 sk;
        asm volatile("global_load_dwordx4 %0, %1, %2" : "=v"(sk) : "v"((unsigned)dch * 8u), "s"((const GAS char*)a.sk));
        float ko[8];
        unsigned kpk[4];
        auto kv_part = [&](int c, int hp) {
            const float w0 = bflo(sk[2 * c]), w1 = bfhi(sk[2 * c]), w2 = bflo(sk[2 * c + 1]), w3 = bfhi(sk[2 * c + 1]);
            float kf[7];
#pragma unroll
            for (int i = 0; i < 7; ++i) {
                unsigned kk = kr[4 * hp + i];
                if (hp == 0 && i < 3) kk = (s0 == 0) ? 0u : kk;
                kf[i] = c ? bfhi(kk) : bflo(kk);
            }
#pragma unroll
            for (int i = 0; i < 4; ++i) {
                float acc = kf[i] * w3;
                acc = fmaf(kf[i + 1], w2, acc);
                acc = fmaf(kf[i + 2], w1, acc);
                acc = fmaf(kf[i + 3], w0, acc);
                ko[4 * hp + i] = acc * (c ? bfhi(vr[4 * hp + i]) : bflo(vr[4 * hp + i]));
            }
            kpk[3 - 2 * hp] = pk2(ko[4 * hp + 1], ko[4 * hp + 0]);
            kpk[2 - 2 * hp] = pk2(ko[4 * hp + 3], ko[4 * hp + 2]);
            if (hp == 1)
                __builtin_amdgcn_raw_buffer_store_b128(u32x4{kpk[0], kpk[1], kpk[2], kpk[3]}, rs, WS_KVR + ((unsigned)(dch + c) * S + (2040 - s0)) * 2, 0, SC1);
        };
        const int wm = w >> 1, wn = w & 1;
        unsigned xa[4], wa[4];
        {
            const int gi = lane >> 4, blk = gi & 1, q4 = (lane >> 2) & 3, p4 = lane & 3, gu = p4 >> 1;
#pragma unroll
            for (int t = 0; t < 4; ++t) xa[t] = (unsigned)(wm * 64 + r) * 128 + (((2 * t + half) ^ ((r >> 1) & 7)) * 16);
#pragma unroll
            for (int i = 0; i < 4; ++i) {
                const int pc = 2 * (4 * wn + i) + blk;
                wa[i] = 16384 + gu * 16384 + (8 * half + q4) * 256 + ((pc ^ ((q4 << 2) | (gu << 1))) * 16) + (p4 & 1) * 8;
            }
        }
        f32x16 acc[4][2];
#pragma unroll
        for (int i = 0; i < 4; ++i)
#pragma unroll
            for (int j = 0; j < 2; ++j)
#pragma unroll
                for (int e = 0; e < 16; ++e) acc[i][j][e] = 0.f;
        s16x8 xf[2][2], wf[2][4];
        auto rdfrag = [&](int ks) {
            const int c = ks >> 2, t = ks & 3, b = ks & 1;
            LAS char* sb = smem + (c % 3) * G1_STAGE;
#pragma unroll
            for (int j = 0; j < 2; ++j) xf[b][j] = *(const LAS s16x8*)(sb + xa[t] + j * 4096);
#pragma unroll
            for (int i = 0; i < 4; ++i) {
                s16x4 lo = __builtin_amdgcn_ds_read_tr16_b64_v4i16((LAS s16x4*)(sb + wa[i] + t * 4096));
                s16x4 hi = __builtin_amdgcn_ds_read_tr16_b64_v4i16((LAS s16x4*)(sb + wa[i] + t * 4096 + 1024));
                wf[b][i] = __builtin_shufflevector(lo, hi, 0, 1, 2, 3, 4, 5, 6, 7);
            }
        };
        float mean[2];
        asm volatile("s_waitcnt vmcnt(0)" : "+v"(kr[0]), "+v"(kr[1]), "+v"(kr[2]), "+v"(kr[3]), "+v"(kr[4]), "+v"(kr[5]), "+v"(kr[6]), "+v"(kr[7]), "+v"(kr[8]), "+v"(kr[9]), "+v"(kr[10]),
                     "+v"(vr[0]), "+v"(vr[1]), "+v"(vr[2]), "+v"(vr[3]), "+v"(vr[4]), "+v"(vr[5]), "+v"(vr[6]), "+v"(vr[7]), "+v"(sk));
        STAMP(24);
        kv_part(0, 0); kv_part(0, 1); kv_part(1, 0); kv_part(1, 1);
        STAMP(25);
        BAR();
        STAMP(1);
        unsigned tch[2] = {0u, 0u};
        rdfrag(0);
#pragma unroll
        for (int ks = 0; ks < 28; ++ks) {
            const int c = ks >> 2, t = ks & 3, b = ks & 1;
            if (t == 3 && c < 6) {
                if (c == 0) asm volatile("s_waitcnt vmcnt(0)" : "+v"(tch[0]), "+v"(tch[1]) :: "memory");
                if (c == 5) STAMP(6);
                BAR();
                if (c == 5) {
#pragma unroll
                    for (int j = 0; j < 2; ++j)
                        mean[j] = __uint_as_float(__builtin_amdgcn_raw_buffer_load_b32(rs, WS_STATS + (unsigned)(rb * 128 + (2 * wm + j) * 32 + r) * 8, 0, SC1));
                }
            }
            if (ks < 27) rdfrag(ks + 1);
#pragma unroll
            for (int i = 0; i < 4; ++i) {
                acc[i][0] = mfma32(wf[b][i], xf[b][0], acc[i][0]);
                acc[i][1] = mfma32(wf[b][i], xf[b][1], acc[i][1]);
            }
#pragma unroll
            for (int i = 0; i < 8; ++i) {
                __builtin_amdgcn_sched_group_barrier(0x008, 1, 0);
                if (i < 5) __builtin_amdgcn_sched_group_barrier(0x100, 2, 0);
            }
            SCHED();
        }
        STAMP(7);
        BAR();
        STAMP(8);
        const LAS float* cs = (const LAS float*)(smem + X_CS);
        f32x4 cva[4][4];
#pragma unroll
        for (int i = 0; i < 4; ++i)
#pragma unroll
            for (int g = 0; g < 4; ++g) cva[i][g] = *(const LAS f32x4*)(cs + (g & 1) * 128 + 16 * (4 * wn + i) + 8 * (g >> 1) + 4 * half);
        SCHED();
#pragma unroll
        for (int i = 0; i < 4; ++i) {
            const int ct = 4 * wn + i;
            const f32x4 (&cv)[4] = cva[i];
#pragma unroll
            for (int j = 0; j < 2; ++j) {
                float hv[8];
#pragma unroll
                for (int e = 0; e < 4; ++e) {
                    float g0 = acc[i][j][e] - mean[j] * cv[0][e];
                    float u0 = acc[i][j][4 + e] - mean[j] * cv[1][e];
                    float g1 = acc[i][j][8 + e] - mean[j] * cv[2][e];
                    float u1 = acc[i][j][12 + e] - mean[j] * cv[3][e];
                    hv[e] = fmaxf(g0, 0.f) * u0;
                    hv[4 + e] = fmaxf(g1, 0.f) * u1;
                }
                u32x4 o = {pk2(hv[0], hv[1]), pk2(hv[2], hv[3]), pk2(hv[4], hv[5]), pk2(hv[6], hv[7])};
                const int T = rb * 4 + 2 * wm + j, KS = cb * 8 + ct;
                __builtin_amdgcn_raw_buffer_store_b128(o, rs, WS_HT + (((unsigned)T * 112 + KS) * 64 + lane) * 16, 0, SC1);
                wdload(2 * i + j);
                SCHED();
            }
        }
        STAMP(9);
    }
    const float Dval = bflo((unsigned)a.Dv[2 * bid + (w >> 2)]);
    BAR();
    STAMP(10);
    const int cw = w >> 2, qp = w & 3;
    f32x4 cacc[8];
#pragma unroll
    for (int c = 0; c < 8; ++c) cacc[c] = f32x4{0.f, 0.f, 0.f, 0.f};
    {
        LAS char* cbp = smem + cw * CH_BYTES;
        const int m = lane & 15, g = lane >> 4;
        const LAS char* ab0 = cbp + (m & 7) * HC_STRIDE + (8 * qp + g + (m >> 3)) * 16;
        const LAS char* gb0 = cbp + 8 * HC_STRIDE + (2032 - 256 * 7 + 64 * qp - 16 * m + 8 * g) * 2;
        u32x4 A[8], G[8], A2[8], G2[8];
#pragma unroll
        for (int p = 0; p < 8; ++p) {
            G[p] = *(const LAS u32x4*)(gb0 + (7 - p) * 512);
            A[p] = *(const LAS u32x4*)(ab0 + p * 512);
        }
        SCHED();
#pragma unroll
        for (int p = 0; p < 8; ++p) {
#pragma unroll
            for (int c = p; c < 8; ++c) cacc[c] = mfma16u(A[p], G[c - p], cacc[c]);
            A2[p] = *(const LAS u32x4*)(ab0 + 64 + p * 512);
            G2[7 - p] = *(const LAS u32x4*)(gb0 + 64 + p * 512);
            if (w < 4) { wdload(8 + 2 * p); wdload(9 + 2 * p); if (p < 4) wdload(24 + p); }
            if (p == 3 && w < 4) {
                WAIT_V(13);
                unsigned old = 0u;
                if (lane == 0) old = __hip_atomic_fetch_add((LAS unsigned*)(smem + X_HC), 1u, __ATOMIC_RELAXED, __HIP_MEMORY_SCOPE_WORKGROUP);
                if (lane == 0 && old == 3u) (void)__hip_atomic_fetch_add(cnt_ptr(a.ws, C_H + rb), 1u, RLX);
            }
            SCHED();
        }
#pragma unroll
        for (int mm = 7; mm >= 0; --mm) {
#pragma unroll
            for (int p = 0; p + mm < 8; ++p) cacc[p + mm] = mfma16u(A2[p], G2[mm], cacc[p + mm]);
        }
    }
    f32x4 keep[2];
    {
        LAS f32x4* red = (LAS f32x4*)(smem + X_CRED) + w * 512;
#pragma unroll
        for (int j = 0; j < 4; ++j) {
            if (qp != j) { red[(2 * j) * 64 + lane] = cacc[2 * j]; red[(2 * j + 1) * 64 + lane] = cacc[2 * j + 1]; }
            else { keep[0] = cacc[2 * j]; keep[1] = cacc[2 * j + 1]; }
        }
    }
    BAR();
    STAMP(11);
    {
        const LAS f32x4* red = (const LAS f32x4*)(smem + X_CRED) + cw * 2048;
        const int ap = lane & 15, g = lane >> 4;
        const LAS char* xrl = smem + cw * CH_BYTES + 8 * HC_STRIDE;
#pragma unroll
        for (int e2 = 0; e2 < 2; ++e2) {
            const int c = 2 * qp + e2;
            f32x4 s = keep[e2];
#pragma unroll
            for (int j = 1; j < 4; ++j) s += red[((qp + j) & 3) * 512 + c * 64 + lane];
            const int s0 = 256 * c + 16 * ap + 4 * g;
            const u32x2 kx = *(const LAS u32x2*)(xrl + (2044 - s0) * 2);
            const float kv0 = bfhi(kx[1]), kv1 = bflo(kx[1]), kv2 = bfhi(kx[0]), kv3 = bflo(kx[0]);
            const float y0 = fmaf(kv0, Dval, s[0]), y1 = fmaf(kv1, Dval, s[1]);
            const float y2 = fmaf(kv2, Dval, s[2]), y3 = fmaf(kv3, Dval, s[3]);
            u32x2 o = {pk2(y0, y1), pk2(y2, y3)};
            __builtin_amdgcn_raw_buffer_store_b64(o, rs, WS_YT + ((unsigned)(2 * bid + cw) * S + s0) * 2, 0, SC1);
        }
    }
    if (lane == 0) poll_ge(cnt_ptr(a.ws, C_H + (rt >> 1)), 14u);
    WAIT_V(0);
    {
        unsigned old = 0u;
        if (lane == 0) old = __hip_atomic_fetch_add((LAS unsigned*)(smem + X_HC + 4), 1u, __ATOMIC_RELAXED, __HIP_MEMORY_SCOPE_WORKGROUP);
        if (lane == 0 && old == 7u) (void)__hip_atomic_fetch_add(cnt_ptr(a.ws, C_Y + ((2 * bid) >> 6)), 1u, RLX);
    }
    STAMP(12);
    u32x4 hw[HWIN][2];
    auto hload = [&](int i) {
        hw[i % HWIN][0] = __builtin_amdgcn_raw_buffer_load_b128(rs, hto, kso(i), LD_AUX);
        hw[i % HWIN][1] = __builtin_amdgcn_raw_buffer_load_b128(rs, hto + 112 * 1024, kso(i), LD_AUX);
    };
#pragma unroll
    for (int i = 0; i < HWIN; ++i) hload(i);
    const int tp = tid >> 4, cq = tid & 15;
    const int tok0 = rt * 64 + 2 * tp, d0 = ctile * 64 + 4 * cq;
    u32x2 q0, q1; u32x4 stu;
    SCHED();
    BAR();
    f32x16 acc2[2][2];
#pragma unroll
    for (int i = 0; i < 2; ++i)
#pragma unroll
        for (int j = 0; j < 2; ++j)
#pragma unroll
            for (int e = 0; e < 16; ++e) acc2[i][j][e] = 0.f;
    unsigned yv[4];
#pragma unroll
    for (int i = 0; i < 14; ++i) {
        const u32x4 hf0 = hw[i % HWIN][0], hf1 = hw[i % HWIN][1];
        if (i + HWIN < 14) hload(i + HWIN);
        if (i == 9) {
            if (lane == 0) poll_ge(cnt_ptr(a.ws, C_Y + ctile), 32u);
            __builtin_amdgcn_fence(__ATOMIC_ACQUIRE, "wavefront");
#pragma unroll
            for (int j = 0; j < 4; ++j)
                yv[j] = __builtin_amdgcn_raw_buffer_load_b32(rs, WS_YT + (unsigned)(d0 + j) * 4096 + tok0 * 2, 0, SC1);
            q0 = *(const u32x2*)(a.q + (size_t)tok0 * D + d0);
            q1 = *(const u32x2*)(a.q + (size_t)(tok0 + 1) * D + d0);
            stu = __builtin_amdgcn_raw_buffer_load_b128(rs, WS_STATS + (unsigned)tok0 * 8, 0, SC1);
        }
        const u32x4 wf0 = wd[i % WWIN][0], wf1 = wd[i % WWIN][1];
        acc2[0][0] = mfma32u(wf0, hf0, acc2[0][0]);
        acc2[0][1] = mfma32u(wf0, hf1, acc2[0][1]);
        acc2[1][0] = mfma32u(wf1, hf0, acc2[1][0]);
        acc2[1][1] = mfma32u(wf1, hf1, acc2[1][1]);
        SCHED();
    }
    STAMP(13);
    {
        LAS char* rbp = smem + w * (64 * RED_STRIDE);
#pragma unroll
        for (int i = 0; i < 2; ++i)
#pragma unroll
            for (int j = 0; j < 2; ++j)
#pragma unroll
                for (int g = 0; g < 4; ++g) {
                    f32x4 v = {acc2[i][j][4 * g], acc2[i][j][4 * g + 1], acc2[i][j][4 * g + 2], acc2[i][j][4 * g + 3]};
                    *(LAS f32x4*)(rbp + (j * 32 + r) * RED_STRIDE + (i * 32 + 8 * g + 4 * half) * 4) = v;
                }
    }
    float xv[2][4], sv[2][4], r2[2];
#pragma unroll
    for (int e = 0; e < 2; ++e) {
        const float mb = __uint_as_float(stu[2 * e]), rd = __uint_as_float(stu[2 * e + 1]);
        const u32x2 qq = e ? q1 : q0;
        r2[e] = rd * rd;
#pragma unroll
        for (int j = 0; j < 4; ++j) {
            const float x = (j & 1) ? bfhi(qq[j >> 1]) : bflo(qq[j >> 1]);
            const float y = e ? bfhi(yv[j]) : bflo(yv[j]);
            xv[e][j] = x; sv[e][j] = (x - mb) * rd * y;
            asm volatile("" : "+v"(xv[e][j]), "+v"(sv[e][j]));
        }
    }
#pragma nounroll
    for (int pass = (DRY && w < 4) ? 0 : 1; pass < 2; ++pass) {
        if (pass == 1) BAR();
        f32x4 o[2];
#pragma unroll
        for (int e = 0; e < 2; ++e) {
            const LAS char* rbp = smem + (2 * tp + e) * RED_STRIDE + cq * 16;
            f32x4 s0 = *(const LAS f32x4*)rbp, s1 = *(const LAS f32x4*)(rbp + 64 * RED_STRIDE);
#pragma unroll
            for (int ww = 2; ww < 8; ww += 2) { s0 += *(const LAS f32x4*)(rbp + ww * (64 * RED_STRIDE)); s1 += *(const LAS f32x4*)(rbp + (ww + 1) * (64 * RED_STRIDE)); }
            o[e] = s0 + s1;
        }
        u32x2 ov2[2];
#pragma unroll
        for (int e = 0; e < 2; ++e) {
            float res[4];
#pragma unroll
            for (int j = 0; j < 4; ++j) {
                const float ov = o[e][j] * r2[e];
                const float gg = ov * __builtin_amdgcn_rcpf(1.0f + __expf(-ov));
                res[j] = fmaf(sv[e][j], gg, xv[e][j]);
            }
            ov2[e] = u32x2{pk2(res[0], res[1]), pk2(res[2], res[3])};
            asm volatile("" : "+v"(ov2[e]));
        }
        if (pass == 1) {
#pragma unroll
            for (int e = 0; e < 2; ++e)
                __builtin_amdgcn_raw_buffer_store_b64(ov2[e], mkrs(a.out, S * D * 2), (unsigned)((tok0 + e) * D + d0) * 2u, 0, SC1);
        }
    }
    STAMP(15);
    STAMP_FLUSH();
}

extern "C" void kernel_launch(void* const* d_in, const int* in_sizes, int n_in,
                              void* d_out, int out_size, void* d_ws, size_t ws_size,
                              hipStream_t stream) {
    (void)hipFuncSetAttribute((const void*)fused_kernel, hipFuncAttributeMaxDynamicSharedMemorySize, LDS_BYTES);
    Args a;
    a.q = (const u16*)d_in[0]; a.k = (const u16*)d_in[1]; a.v = (const u16*)d_in[2]; a.sk = (const u16*)d_in[3];
    a.ssm = (const u16*)d_in[4]; a.Dv = (const u16*)d_in[5];
    a.wg = (const u16*)d_in[7]; a.wu = (const u16*)d_in[8]; a.wd = (const u16*)d_in[9];
    a.ws = (char*)d_ws; a.out = (u16*)d_out;
    fused_kernel<<<NMAIN + NPREP + 1, 512, LDS_BYTES, stream>>>(a);
}
```

```cpp
#include <hip/hip_runtime.h>
#include <stdint.h>

typedef unsigned short u16;
typedef __attribute__((ext_vector_type(4))) short s16x4;
typedef __attribute__((ext_vector_type(8))) short s16x8;
typedef __attribute__((ext_vector_type(8))) __bf16 bf16x8;
typedef __attribute__((ext_vector_type(2))) __bf16 bf16x2;
typedef __attribute__((ext_vector_type(16))) float f32x16;
typedef __attribute__((ext_vector_type(4))) float f32x4;
typedef __attribute__((ext_vector_type(4))) unsigned u32x4;
typedef __attribute__((ext_vector_type(2))) unsigned u32x2;
#define GAS __attribute__((address_space(1)))
#define LAS __attribute__((address_space(3)))
#define DI __device__ __forceinline__
typedef GAS unsigned gu32;
typedef GAS unsigned long long gu64;
typedef unsigned long long u64;
#define RLX __ATOMIC_RELAXED, __HIP_MEMORY_SCOPE_AGENT

static constexpr int S = 2048, D = 448, F = 1792;
static constexpr int NMAIN = 224, NPREP = 28;
#define STAMP_DECL
#define STAMP(i)
#define STAMP_FLUSH()

static constexpr unsigned SY_M = 0, SY_A = 2048, SY_CNT = 4096;
static constexpr int C_KV = 0, C_ST = 7, C_HS = 21, C_WD = 22, C_H = 29, C_Y = 45, C_CS = 52, C_N = 59;
static constexpr unsigned WS_STATS = 16384;
static constexpr unsigned WS_CS    = WS_STATS + S * 8;
static constexpr unsigned WS_HT    = WS_CS + 16384;
static constexpr unsigned WS_WDT   = WS_HT + S * F * 2;
static constexpr unsigned WS_KVR   = WS_WDT + F * D * 2;
static constexpr unsigned WS_YT    = WS_KVR + S * D * 2;
static constexpr unsigned WS_END   = WS_YT + S * D * 2;
static constexpr int SC1 = 16;
#define KVDELAY 0
#define SDELAY 0
#define DRY 0
#define ROT1 0
#define PAGE_TOUCH 0
#define CODE_LINES 0
#define PRE 8
#define HDELAY 3
#define TCH0 7
#define LD_AUX 16
static constexpr unsigned SPIN_CAP = 1u << 16;

DI float bflo(unsigned u) { return __uint_as_float(u << 16); }
DI float bfhi(unsigned u) { return __uint_as_float(u & 0xffff0000u); }
DI unsigned pk2(float a, float b) { bf16x2 r = {(__bf16)a, (__bf16)b}; return __builtin_bit_cast(unsigned, r); }
DI float rbf(float a) { return bflo(pk2(a, a)); }
template <int CTRL> DI float dpp_add(float v) {
    return v + __int_as_float(__builtin_amdgcn_update_dpp(0, __float_as_int(v), CTRL, 0xf, 0xf, true));
}
DI float sum8(float v) { v = dpp_add<0xB1>(v); v = dpp_add<0x4E>(v); return dpp_add<0x141>(v); }
DI float xor16_add(float v) { auto r = __builtin_amdgcn_permlane16_swap(__float_as_uint(v), __float_as_uint(v), false, false); return __uint_as_float(r[0]) + __uint_as_float(r[1]); }
DI float xor32_add(float v) { auto r = __builtin_amdgcn_permlane32_swap(__float_as_uint(v), __float_as_uint(v), false, false); return __uint_as_float(r[0]) + __uint_as_float(r[1]); }
DI float sum32(float v) { v = sum8(v); v = dpp_add<0x140>(v); return xor16_add(v); }
DI f32x16 mfma32(s16x8 a, s16x8 b, f32x16 c) {
    return __builtin_amdgcn_mfma_f32_32x32x16_bf16(__builtin_bit_cast(bf16x8, a), __builtin_bit_cast(bf16x8, b), c, 0, 0, 0);
}
DI f32x4 mfma16u(u32x4 a, u32x4 b, f32x4 c) {
    return __builtin_amdgcn_mfma_f32_16x16x32_bf16(__builtin_bit_cast(bf16x8, a), __builtin_bit_cast(bf16x8, b), c, 0, 0, 0);
}
DI f32x16 mfma32u(u32x4 a, u32x4 b, f32x16 c) { return mfma32(__builtin_bit_cast(s16x8, a), __builtin_bit_cast(s16x8, b), c); }
#define WAIT_V(n) asm volatile("s_waitcnt vmcnt(" #n ")" ::: "memory")
#define BAR() do { asm volatile("s_waitcnt lgkmcnt(0)" ::: "memory"); __builtin_amdgcn_s_barrier(); asm volatile("" ::: "memory"); } while (0)
#define SCHED() __builtin_amdgcn_sched_barrier(0)
DI __amdgpu_buffer_rsrc_t mkrs(const void* p, unsigned bytes) { return __builtin_amdgcn_make_buffer_rsrc((void*)p, 0, bytes, 0x00020000); }
DI gu32* cnt_ptr(char* ws, int i) { return (gu32*)(ws + SY_CNT + 128 * i); }
DI void poll_eq(gu32* p, unsigned want) {
    for (unsigned sp = 0; sp < SPIN_CAP; ++sp) { if (__hip_atomic_load(p, RLX) == want) break; __builtin_amdgcn_s_sleep(1); }
}
DI void poll_eq64(gu64* p, u64 want) {
    for (unsigned sp = 0; sp < SPIN_CAP; ++sp) { if (__hip_atomic_load(p, RLX) == want) break; __builtin_amdgcn_s_sleep(1); }
}
DI void poll_ge(gu32* p, unsigned want) {
    for (unsigned sp = 0; sp < SPIN_CAP; ++sp) { if (__hip_atomic_load(p, RLX) >= want) break; __builtin_amdgcn_s_sleep(1); }
}

struct Args {
    const u16* q; const u16* k; const u16* v; const u16* sk; const u16* ssm; const u16* Dv;
    const u16* wg; const u16* wu; const u16* wd;
    char* ws; u16* out;
};

static constexpr int G1_STAGE = 49152;
static constexpr int X_CS = 3 * G1_STAGE;
static constexpr int HC_STRIDE = 4160;
static constexpr int CH_BYTES = 8 * HC_STRIDE + 4608;
static constexpr int X_CRED = 2 * CH_BYTES;
static constexpr int RED_STRIDE = 272;
static constexpr int X_HC = X_CS + 1024;
static constexpr int X_SC = X_HC + 16;
static constexpr int LDS_BYTES = X_SC + 8192;
static constexpr int WWIN = 14;
static constexpr int HWIN = 6;

DI void dma16(const GAS char* sbase, unsigned voff, unsigned ldst) {
    unsigned keep;
    asm volatile("s_mov_b32 %0, m0\n\ts_mov_b32 m0, %3\n\ts_nop 0\n\tglobal_load_lds_dwordx4 %1, %2\n\ts_mov_b32 m0, %0"
                 : "=&s"(keep) : "v"(voff), "s"(sbase), "s"(ldst));
}

DI void service_block(const Args& a, LAS char* smem) {
    const int lane = threadIdx.x & 63, w = threadIdx.x >> 6;
    const __amdgpu_buffer_rsrc_t rs = mkrs(a.ws, WS_END);
    volatile LAS unsigned* stop = (volatile LAS unsigned*)smem;
    if (w == 0) {
        if (lane < C_N) __hip_atomic_store(cnt_ptr(a.ws, lane), 0u, RLX);
        if (lane == 0) *stop = 0u;
        WAIT_V(0);
    }
    BAR();
    if (w == 0) {
        for (unsigned it = 0; it < SPIN_CAP; ++it) {
            const unsigned c = __hip_atomic_load(cnt_ptr(a.ws, lane < C_N ? lane : 0), RLX);
            const bool ok = (lane >= C_CS && lane < C_N) ? (c >= 4u) : (lane < C_ST) ? (c >= 32u) : true;
            if (__all(ok)) break;
            __builtin_amdgcn_s_sleep(8);
        }
        if (lane == 0) *stop = 1u;
    } else if (w < 3) {
        for (int i = 0; i < (w - 1) * 4; ++i) __builtin_amdgcn_s_sleep(2);
        for (unsigned it = 0; it < SPIN_CAP; ++it) {
            const u32x4 m0 = __builtin_amdgcn_raw_buffer_load_b128(rs, SY_M + lane * 32, 0, SC1);
            const u32x4 m1 = __builtin_amdgcn_raw_buffer_load_b128(rs, SY_M + lane * 32 + 16, 0, SC1);
            __builtin_amdgcn_raw_buffer_store_b128(m0, rs, SY_A + lane * 32, 0, SC1);
            __builtin_amdgcn_raw_buffer_store_b128(m1, rs, SY_A + lane * 32 + 16, 0, SC1);
            if (*stop) break;
            __builtin_amdgcn_s_sleep(6);
        }
    }
}

DI u32x2 row_stats(const u32x4 (&xs)[7]) {
    float s0 = 0.f, s1 = 0.f;
#pragma unroll
    for (int i = 0; i < 7; ++i)
#pragma unroll
        for (int j = 0; j < 4; ++j) { s0 += bflo(xs[i][j]); s1 += bfhi(xs[i][j]); }
    float sum = s0 + s1;
    sum = sum8(sum);
    const float m = sum * (1.0f / D);
    float q0 = 0.f, q1 = 0.f;
#pragma unroll
    for (int i = 0; i < 7; ++i)
#pragma unroll
        for (int j = 0; j < 4; ++j) {
            const float d0 = bflo(xs[i][j]) - m, d1 = bfhi(xs[i][j]) - m;
            q0 = fmaf(d0, d0, q0); q1 = fmaf(d1, d1, q1);
        }
    float sq = q0 + q1;
    sq = sum8(sq);
    const float var = sq * (1.0f / D);
    const float mb = rbf(m), vb = rbf(var);
    const float den = rbf(sqrtf(rbf(vb + 1e-5f)));
    return u32x2{__float_as_uint(mb), __float_as_uint(1.0f / den)};
}

#define PSTAMP(i)
DI void prep_block(const Args& a, LAS char* smem) {
    const int tid = threadIdx.x, lane = tid & 63;
    const int w = __builtin_amdgcn_readfirstlane(tid >> 6);
    const int g = (blockIdx.x - NMAIN) * 8 + w;
    const __amdgpu_buffer_rsrc_t rs = mkrs(a.ws, WS_END);
    gu64* mbA = (gu64*)(a.ws + SY_A) + blockIdx.x;
    const u64 chal = __builtin_amdgcn_s_memrealtime();
    if (tid == 0) __hip_atomic_store((gu64*)(a.ws + SY_M) + blockIdx.x, chal, RLX);
    for (int i = 0; i < HDELAY; ++i) __builtin_amdgcn_s_sleep(16);
    const int hb = blockIdx.x - NMAIN;
    const u16* wsrc = (hb < 14 ? a.wg : a.wu) + (hb < 14 ? hb : hb - 14) * 128 + (tid & 15) * 8;
    u32x4 wr[14];
#pragma unroll
    for (int i = 0; i < 14; ++i) wr[i] = *(const u32x4*)(wsrc + (size_t)((tid >> 4) * 14 + i) * F);
    u32x4 xs[7];
    if (g < 32) {
        const int srow = (224 + g) * 8 + (lane >> 3);
#pragma unroll
        for (int i = 0; i < 7; ++i) xs[i] = *(const u32x4*)(a.q + (size_t)srow * D + ((lane & 7) + 8 * i) * 8);
    }
    SCHED();
    PSTAMP(1);
    float cs[8];
#pragma unroll
    for (int j = 0; j < 8; ++j) cs[j] = 0.f;
#pragma unroll
    for (int i = 0; i < 14; ++i)
#pragma unroll
        for (int e = 0; e < 4; ++e) { cs[2 * e] += bflo(wr[i][e]); cs[2 * e + 1] += bfhi(wr[i][e]); }
#pragma unroll
    for (int j = 0; j < 8; ++j) cs[j] = xor32_add(xor16_add(cs[j]));
    LAS f32x4* red = (LAS f32x4*)smem;
    if (lane < 16) { red[(w * 16 + lane) * 2] = f32x4{cs[0], cs[1], cs[2], cs[3]}; red[(w * 16 + lane) * 2 + 1] = f32x4{cs[4], cs[5], cs[6], cs[7]}; }
    if (g < 32) {
        const u32x2 o = row_stats(xs);
        if ((lane & 7) == 0) __builtin_amdgcn_raw_buffer_store_b64(o, rs, WS_STATS + (unsigned)((224 + g) * 8 + (lane >> 3)) * 8, 0, SC1);
    }
    BAR();
    if (tid < 32) {
        f32x4 s = red[tid];
#pragma unroll
        for (int ww = 1; ww < 8; ++ww) s += red[ww * 32 + tid];
        __builtin_amdgcn_raw_buffer_store_b128(__builtin_bit_cast(u32x4, s), rs, WS_CS + (unsigned)((hb < 14 ? 0 : 1792) + (hb < 14 ? hb : hb - 14) * 128 + tid * 4) * 4, 0, SC1);
    }
    PSTAMP(4);
    WAIT_V(0);
    PSTAMP(5);
    BAR();
    PSTAMP(6);
    if (w == 0) {
        if (lane == 0) poll_eq64(mbA, chal);
        const int cbh = hb < 14 ? hb : hb - 14;
        if (lane == 0 || (lane == 1 && hb < 4)) (void)__hip_atomic_fetch_add(cnt_ptr(a.ws, lane == 0 ? C_CS + (cbh >> 1) : C_HS), 1u, RLX);
    }
}

__global__ __launch_bounds__(512, 2) void fused_kernel(Args a) {
    extern __shared__ __attribute__((aligned(16))) char smem_[];
    LAS char* smem = (LAS char*)smem_;
    const int bid = blockIdx.x;
    u64 kernel_pc;
    asm volatile("s_getpc_b64 %0" : "=s"(kernel_pc));
    STAMP_DECL
    STAMP(0);
    if (bid >= NMAIN) {
        if (bid == NMAIN + NPREP) service_block(a, smem); else prep_block(a, smem);
        STAMP(15); STAMP_FLUSH();
        return;
    }
    const int tid = threadIdx.x, lane = tid & 63;
    const int w = __builtin_amdgcn_readfirstlane(tid >> 6);
    const int xcd = bid & 7, idx = bid >> 3;
    const __amdgpu_buffer_rsrc_t rs = mkrs(a.ws, WS_END);
    const int rb = (xcd >> 1) * 4 + (idx & 3);
    const int cb = (xcd & 1) * 7 + (idx >> 2);
    const int rt = xcd * 4 + (idx & 3);
    const int ctile = idx >> 2;
    const int r = lane & 31, half = lane >> 5;
    const int rot = (2 * ctile + 3 * (idx & 3) + (idx & 2) / 2) % 14;
    const unsigned wdo = WS_WDT + (((unsigned)(ctile * 2) * 112 + 14 * w) * 64 + lane) * 16;
    const unsigned hto = WS_HT + (((unsigned)(rt * 2) * 112 + 14 * w) * 64 + lane) * 16;
    auto kso = [&](int i) { const int k = i + rot; return (unsigned)(k >= 14 ? k - 14 : k) * 1024u; };
    u32x4 wd[WWIN][2];
    auto wdload = [&](int n) {
        wd[(n >> 1) % WWIN][n & 1] = __builtin_amdgcn_raw_buffer_load_b128(rs, wdo + (n & 1) * 112 * 1024, kso(n >> 1), LD_AUX);
    };
    if (w >= 4) {
        const int lw = w - 4, lt = tid - 256;
        u32x4 xr[2], h0[2], h1[2];
        bool wd_early = false;
        auto taps = [&]() {
#pragma unroll
            for (int c2 = 0; c2 < 2; ++c2) {
                const u16* hs = a.ssm + (size_t)(2 * bid + c2) * S;
                h0[c2] = *(const u32x4*)(hs + (lt > 0 ? lt - 1 : 0) * 8);
                h1[c2] = *(const u32x4*)(hs + lt * 8);
            }
        };
        if (lw < 3) {
            const GAS char* gsrc; unsigned vo[2]; size_t cstep, istep; unsigned ldst;
            if (lw < 2) {
                gsrc = (const GAS char*)(lw ? a.wu : a.wg) + (size_t)cb * 256;
                vo[0] = vo[1] = (lane >> 4) * (F * 2) + (((lane & 15) ^ (((lane >> 4) << 2) | (lw << 1))) * 16);
                cstep = (size_t)64 * (F * 2); istep = (size_t)8 * (F * 2);
                ldst = 16384 + lw * 16384;
            } else {
                gsrc = (const GAS char*)a.q + (size_t)rb * 128 * D * 2;
#pragma unroll
                for (int e = 0; e < 2; ++e) { const int row = 8 * e + (lane >> 3); vo[e] = row * (D * 2) + (((lane & 7) ^ ((row >> 1) & 7)) * 16); }
                cstep = 128; istep = (size_t)16 * (D * 2);
                ldst = 0;
            }
            if (lw < 2) vo[1] += 4 * (F * 2);
            const unsigned lds0 = (unsigned)(uintptr_t)smem + ldst;
            const int rot1 = ROT1 ? ((idx >> 2) + 2 * (idx & 3)) % 7 : 0;
            auto pieces = [&](int c, int i0, int i1) {
                const int cc = c + rot1 >= 7 ? c + rot1 - 7 : c + rot1;
#pragma unroll
                for (int i = i0; i < i1; ++i)
                    dma16(gsrc + cc * cstep + (i >> 1) * istep, vo[i & 1], lds0 + (c % 3) * G1_STAGE + i * 1024);
            };
            auto stage = [&](int c) { pieces(c, 0, 16); };
            STAMP(24);
            stage(0);
            STAMP(25);
            pieces(1, 0, PRE);
            STAMP(26);
            asm volatile("s_waitcnt vmcnt(%0)" :: "n"(PRE) : "memory");
            BAR();
            STAMP(1);
            pieces(1, PRE, 16);
#pragma unroll
            for (int c = 0; c < 6; ++c) {
                if (c + 2 <= 6) stage(c + 2);
                asm volatile("" ::: "memory");
                SCHED();
                if (c == 4) taps();
                if (c == 5) {
                    wd_early = __builtin_amdgcn_readfirstlane(*(const LAS unsigned*)(smem + X_HC + 8)) != 0;
                    WAIT_V(4);
                    if (wd_early) {
#pragma unroll
                        for (int n = 0; n < 10; ++n) wdload(n);
                    }
                }
                SCHED();
                STAMP(16 + c);
                if (c == 4) WAIT_V(20);
                else if (c == 5) {}
                else WAIT_V(16);
                BAR();
                STAMP(2 + c);
            }
        } else {
            gu64* mbA = (gu64*)(a.ws + SY_A) + bid;
            const u64 chal = __builtin_amdgcn_s_memrealtime();
            if (lane == 0) __hip_atomic_store((gu64*)(a.ws + SY_M) + bid, chal, RLX);
            if (SDELAY) __builtin_amdgcn_s_sleep(SDELAY);
            u32x4 wt[7], xs[7];
#pragma unroll
            for (int e = 0; e < 7; ++e) {
                const int t = 7 * bid + e, dt = t / 112, ks = t % 112;
                wt[e] = *(const u32x4*)(a.wd + (size_t)(16 * ks + (lane >> 2)) * D + 32 * dt + (lane & 3) * 8);
            }
#pragma unroll
            for (int i = 0; i < 7; ++i) xs[i] = *(const u32x4*)(a.q + (size_t)(8 * bid + (lane >> 3)) * D + ((lane & 7) + 8 * i) * 8);
            if (lane == 0) { *(LAS unsigned*)(smem + X_HC) = 0u; *(LAS unsigned*)(smem + X_HC + 4) = 0u; }
            SCHED();
            BAR();
            STAMP(1);
            u64 ans = __hip_atomic_load(mbA, RLX);
            {
                LAS char* sc = smem + X_SC;
                const int gi = lane >> 4, hf = gi >> 1, blk = gi & 1, q4 = (lane >> 2) & 3, p4 = lane & 3;
                const int ra = (4 * hf + q4) * 64 + (16 * blk + 4 * p4) * 2;
#pragma unroll
                for (int e = 0; e < 7; ++e) *(LAS u32x4*)(sc + e * 1024 + lane * 16) = wt[e];
                asm volatile("s_waitcnt lgkmcnt(0)" ::: "memory");
#pragma unroll
                for (int e = 0; e < 7; ++e) {
                    s16x4 lo = __builtin_amdgcn_ds_read_tr16_b64_v4i16((LAS s16x4*)(sc + e * 1024 + ra));
                    s16x4 hi = __builtin_amdgcn_ds_read_tr16_b64_v4i16((LAS s16x4*)(sc + e * 1024 + ra + 512));
                    s16x8 o = __builtin_shufflevector(lo, hi, 0, 1, 2, 3, 4, 5, 6, 7);
                    __builtin_amdgcn_raw_buffer_store_b128(__builtin_bit_cast(u32x4, o), rs, WS_WDT + (unsigned)(7 * bid + e) * 1024 + lane * 16, 0, SC1);
                }
            }
            STAMP(16);
            BAR();
            STAMP(2);
            if (ans != chal) { if (lane == 0) poll_eq64(mbA, chal); }
            if (lane == 0) (void)__hip_atomic_fetch_add(cnt_ptr(a.ws, C_KV + bid % 7), 1u, RLX);
            STAMP(22);
            {
                const u32x2 o = row_stats(xs);
                if ((lane & 7) == 0) __builtin_amdgcn_raw_buffer_store_b64(o, rs, WS_STATS + (unsigned)(8 * bid + (lane >> 3)) * 8, 0, SC1);
            }
            STAMP(17);
            BAR();
            STAMP(3);
            WAIT_V(0);
            if (lane < 2) (void)__hip_atomic_fetch_add(cnt_ptr(a.ws, lane == 0 ? C_WD + (bid >> 5) : C_ST + (bid >> 4)), 1u, RLX);
            STAMP(23);
            BAR();
            STAMP(4);
            taps();
            BAR();
            STAMP(5);
            const int ci = (lane == 1) ? (rb < 14 ? C_ST + rb : C_HS) : (lane == 2) ? C_WD + ctile : (lane == 3) ? C_CS + (cb >> 1) : C_KV + (bid >> 5);
            const unsigned want = (lane == 1) ? (rb < 14 ? 16u : 4u) : (lane == 3) ? 4u : 32u;
            unsigned cnt = __hip_atomic_load(cnt_ptr(a.ws, ci), RLX);
            wd_early = __builtin_amdgcn_readlane(cnt, 2) >= 32;
            if (lane == 0) *(LAS unsigned*)(smem + X_HC + 8) = wd_early ? 1u : 0u;
            BAR();
            STAMP(6);
            {
                if (!__all(cnt >= want)) cnt = __hip_atomic_load(cnt_ptr(a.ws, ci), RLX);
                if (wd_early) {
#pragma unroll
                    for (int n = 0; n < 10; ++n) wdload(n);
                }
                for (unsigned sp = 0; sp < SPIN_CAP; ++sp) {
                    if (__all(cnt >= want)) break;
                    __builtin_amdgcn_s_sleep(1);
                    cnt = __hip_atomic_load(cnt_ptr(a.ws, ci), RLX);
                }
            }
            STAMP(21);
            BAR();
            STAMP(7);
        }
        const u32x4 cv = __builtin_amdgcn_raw_buffer_load_b128(rs, WS_CS + (unsigned)((lane >> 5) * 1792 + cb * 128 + (lane & 31) * 4) * 4, 0, SC1);
#pragma unroll
        for (int c2 = 0; c2 < 2; ++c2)
            xr[c2] = __builtin_amdgcn_raw_buffer_load_b128(rs, WS_KVR + ((unsigned)(2 * bid + c2) * S + lt * 8) * 2, 0, SC1);
        if (!wd_early) {
#pragma unroll
            for (int n = 0; n < 10; ++n) wdload(n);
        }
#pragma unroll
        for (int n = 10; n < 28; ++n) wdload(n);
        if (lw == 0) *(LAS u32x4*)(smem + X_CS + lane * 16) = cv;
        BAR();
        STAMP(8);
#pragma unroll
        for (int c2 = 0; c2 < 2; ++c2) {
            LAS char* cbp = smem + c2 * CH_BYTES;
            u32x4 hh0 = h0[c2];
            if (lt == 0) hh0 = u32x4{0, 0, 0, 0};
            const unsigned d[8] = {hh0[0], hh0[1], hh0[2], hh0[3], h1[c2][0], h1[c2][1], h1[c2][2], h1[c2][3]};
            unsigned al[7];
#pragma unroll
            for (int i = 0; i < 7; ++i) al[i] = __builtin_amdgcn_alignbit(d[i + 1], d[i], 16);
            const int t = lt + 1;
#pragma unroll
            for (int sg = 0; sg < 8; ++sg) {
                const int sh = sg + 1;
                u32x4 o;
#pragma unroll
                for (int e = 0; e < 4; ++e) o[e] = (sh & 1) ? al[(sh >> 1) + e] : d[(sh >> 1) + e];
                *(LAS u32x4*)(cbp + sg * HC_STRIDE + t * 16) = o;
            }
            if (lt < 8) *(LAS u32x4*)(cbp + lt * HC_STRIDE) = u32x4{0, 0, 0, 0};
            *(LAS u32x4*)(cbp + 8 * HC_STRIDE + lt * 16) = xr[c2];
            if (lt < 32) *(LAS u32x4*)(cbp + 8 * HC_STRIDE + 4096 + lt * 16) = u32x4{0, 0, 0, 0};
        }
        STAMP(9);
    } else {
        const int s0 = (bid / 7) * 64 + (2 * w + half) * 8;
        const int dch = (bid % 7) * 64 + 2 * r;
        if (KVDELAY) __builtin_amdgcn_s_sleep(KVDELAY);
        unsigned kr[11], vr[8];
#pragma unroll
        for (int i = 0; i < 11; ++i) {
            const int s = s0 - 3 + i;
            asm volatile("global_load_dword %0, %1, %2" : "=v"(kr[i]) : "v"((unsigned)((s < 0 ? 0 : s) * D + dch) * 2u), "s"((const GAS char*)a.k));
        }
#pragma unroll
        for (int i = 0; i < 8; ++i)
            asm volatile("global_load_dword %0, %1, %2" : "=v"(vr[i]) : "v"((unsigned)((s0 + i) * D + dch) * 2u), "s"((const GAS char*)a.v));
        u32x4 sk;
        asm volatile("global_load_dwordx4 %0, %1, %2" : "=v"(sk) : "v"((unsigned)dch * 8u), "s"((const GAS char*)a.sk));
        float ko[8];
        unsigned kpk[4];
        auto kv_part = [&](int c, int hp) {
            const float w0 = bflo(sk[2 * c]), w1 = bfhi(sk[2 * c]), w2 = bflo(sk[2 * c + 1]), w3 = bfhi(sk[2 * c + 1]);
            float kf[7];
#pragma unroll
            for (int i = 0; i < 7; ++i) {
                unsigned kk = kr[4 * hp + i];
                if (hp == 0 && i < 3) kk = (s0 == 0) ? 0u : kk;
                kf[i] = c ? bfhi(kk) : bflo(kk);
            }
#pragma unroll
            for (int i = 0; i < 4; ++i) {
                float acc = kf[i] * w3;
                acc = fmaf(kf[i + 1], w2, acc);
                acc = fmaf(kf[i + 2], w1, acc);
                acc = fmaf(kf[i + 3], w0, acc);
                ko[4 * hp + i] = acc * (c ? bfhi(vr[4 * hp + i]) : bflo(vr[4 * hp + i]));
            }
            kpk[3 - 2 * hp] = pk2(ko[4 * hp + 1], ko[4 * hp + 0]);
            kpk[2 - 2 * hp] = pk2(ko[4 * hp + 3], ko[4 * hp + 2]);
            if (hp == 1)
                __builtin_amdgcn_raw_buffer_store_b128(u32x4{kpk[0], kpk[1], kpk[2], kpk[3]}, rs, WS_KVR + ((unsigned)(dch + c) * S + (2040 - s0)) * 2, 0, SC1);
        };
        const int wm = w >> 1, wn = w & 1;
        unsigned xa[4], wa[4];
        {
            const int gi = lane >> 4, blk = gi & 1, q4 = (lane >> 2) & 3, p4 = lane & 3, gu = p4 >> 1;
#pragma unroll
            for (int t = 0; t < 4; ++t) xa[t] = (unsigned)(wm * 64 + r) * 128 + (((2 * t + half) ^ ((r >> 1) & 7)) * 16);
#pragma unroll
            for (int i = 0; i < 4; ++i) {
                const int pc = 2 * (4 * wn + i) + blk;
                wa[i] = 16384 + gu * 16384 + (8 * half + q4) * 256 + ((pc ^ ((q4 << 2) | (gu << 1))) * 16) + (p4 & 1) * 8;
            }
        }
        f32x16 acc[4][2];
#pragma unroll
        for (int i = 0; i < 4; ++i)
#pragma unroll
            for (int j = 0; j < 2; ++j)
#pragma unroll
                for (int e = 0; e < 16; ++e) acc[i][j][e] = 0.f;
        s16x8 xf[2][2], wf[2][4];
        auto rdfrag = [&](int ks) {
            const int c = ks >> 2, t = ks & 3, b = ks & 1;
            LAS char* sb = smem + (c % 3) * G1_STAGE;
#pragma unroll
            for (int j = 0; j < 2; ++j) xf[b][j] = *(const LAS s16x8*)(sb + xa[t] + j * 4096);
#pragma unroll
            for (int i = 0; i < 4; ++i) {
                s16x4 lo = __builtin_amdgcn_ds_read_tr16_b64_v4i16((LAS s16x4*)(sb + wa[i] + t * 4096));
                s16x4 hi = __builtin_amdgcn_ds_read_tr16_b64_v4i16((LAS s16x4*)(sb + wa[i] + t * 4096 + 1024));
                wf[b][i] = __builtin_shufflevector(lo, hi, 0, 1, 2, 3, 4, 5, 6, 7);
            }
        };
        float mean[2];
        asm volatile("s_waitcnt vmcnt(0)" : "+v"(kr[0]), "+v"(kr[1]), "+v"(kr[2]), "+v"(kr[3]), "+v"(kr[4]), "+v"(kr[5]), "+v"(kr[6]), "+v"(kr[7]), "+v"(kr[8]), "+v"(kr[9]), "+v"(kr[10]),
                     "+v"(vr[0]), "+v"(vr[1]), "+v"(vr[2]), "+v"(vr[3]), "+v"(vr[4]), "+v"(vr[5]), "+v"(vr[6]), "+v"(vr[7]), "+v"(sk));
        STAMP(24);
        kv_part(0, 0); kv_part(0, 1); kv_part(1, 0); kv_part(1, 1);
        STAMP(25);
        BAR();
        STAMP(1);
        unsigned tch[2] = {0u, 0u};
        rdfrag(0);
#pragma unroll
        for (int ks = 0; ks < 28; ++ks) {
            const int c = ks >> 2, t = ks & 3, b = ks & 1;
            if (t == 3 && c < 6) {
                if (c == 0) asm volatile("s_waitcnt vmcnt(0)" : "+v"(tch[0]), "+v"(tch[1]) :: "memory");
                if (c == 5) STAMP(6);
                BAR();
                if (c == 5) {
#pragma unroll
                    for (int j = 0; j < 2; ++j)
                        mean[j] = __uint_as_float(__builtin_amdgcn_raw_buffer_load_b32(rs, WS_STATS + (unsigned)(rb * 128 + (2 * wm + j) * 32 + r) * 8, 0, SC1));
                }
            }
            if (ks < 27) rdfrag(ks + 1);
#pragma unroll
            for (int i = 0; i < 4; ++i) {
                acc[i][0] = mfma32(wf[b][i], xf[b][0], acc[i][0]);
                acc[i][1] = mfma32(wf[b][i], xf[b][1], acc[i][1]);
            }
#pragma unroll
            for (int i = 0; i < 8; ++i) {
                __builtin_amdgcn_sched_group_barrier(0x008, 1, 0);
                if (i < 5) __builtin_amdgcn_sched_group_barrier(0x100, 2, 0);
            }
            SCHED();
        }
        STAMP(7);
        BAR();
        STAMP(8);
        const LAS float* cs = (const LAS float*)(smem + X_CS);
        f32x4 cva[4][4];
#pragma unroll
        for (int i = 0; i < 4; ++i)
#pragma unroll
            for (int g = 0; g < 4; ++g) cva[i][g] = *(const LAS f32x4*)(cs + (g & 1) * 128 + 16 * (4 * wn + i) + 8 * (g >> 1) + 4 * half);
        SCHED();
#pragma unroll
        for (int i = 0; i < 4; ++i) {
            const int ct = 4 * wn + i;
            const f32x4 (&cv)[4] = cva[i];
#pragma unroll
            for (int j = 0; j < 2; ++j) {
                float hv[8];
#pragma unroll
                for (int e = 0; e < 4; ++e) {
                    float g0 = acc[i][j][e] - mean[j] * cv[0][e];
                    float u0 = acc[i][j][4 + e] - mean[j] * cv[1][e];
                    float g1 = acc[i][j][8 + e] - mean[j] * cv[2][e];
                    float u1 = acc[i][j][12 + e] - mean[j] * cv[3][e];
                    hv[e] = fmaxf(g0, 0.f) * u0;
                    hv[4 + e] = fmaxf(g1, 0.f) * u1;
                }
                u32x4 o = {pk2(hv[0], hv[1]), pk2(hv[2], hv[3]), pk2(hv[4], hv[5]), pk2(hv[6], hv[7])};
                const int T = rb * 4 + 2 * wm + j, KS = cb * 8 + ct;
                __builtin_amdgcn_raw_buffer_store_b128(o, rs, WS_HT + (((unsigned)T * 112 + KS) * 64 + lane) * 16, 0, SC1);
                wdload(2 * i + j);
                SCHED();
            }
        }
        STAMP(9);
    }
    const float Dval = bflo((unsigned)a.Dv[2 * bid + (w >> 2)]);
    BAR();
    STAMP(10);
    const int cw = w >> 2, qp = w & 3;
    f32x4 cacc[8];
#pragma unroll
    for (int c = 0; c < 8; ++c) cacc[c] = f32x4{0.f, 0.f, 0.f, 0.f};
    {
        LAS char* cbp = smem + cw * CH_BYTES;
        const int m = lane & 15, g = lane >> 4;
        const LAS char* ab0 = cbp + (m & 7) * HC_STRIDE + (8 * qp + g + (m >> 3)) * 16;
        const LAS char* gb0 = cbp + 8 * HC_STRIDE + (2032 - 256 * 7 + 64 * qp - 16 * m + 8 * g) * 2;
        u32x4 A[8], G[8], A2[8], G2[8];
#pragma unroll
        for (int p = 0; p < 8; ++p) {
            G[p] = *(const LAS u32x4*)(gb0 + (7 - p) * 512);
            A[p] = *(const LAS u32x4*)(ab0 + p * 512);
        }
        SCHED();
#pragma unroll
        for (int p = 0; p < 8; ++p) {
#pragma unroll
            for (int c = p; c < 8; ++c) cacc[c] = mfma16u(A[p], G[c - p], cacc[c]);
            A2[p] = *(const LAS u32x4*)(ab0 + 64 + p * 512);
            G2[7 - p] = *(const LAS u32x4*)(gb0 + 64 + p * 512);
            if (w < 4) { wdload(8 + 2 * p); wdload(9 + 2 * p); if (p < 4) wdload(24 + p); }
            if (p == 3 && w < 4) {
                WAIT_V(13);
                unsigned old = 0u;
                if (lane == 0) old = __hip_atomic_fetch_add((LAS unsigned*)(smem + X_HC), 1u, __ATOMIC_RELAXED, __HIP_MEMORY_SCOPE_WORKGROUP);
                if (lane == 0 && old == 3u) (void)__hip_atomic_fetch_add(cnt_ptr(a.ws, C_H + rb), 1u, RLX);
            }
            SCHED();
        }
#pragma unroll
        for (int mm = 7; mm >= 0; --mm) {
#pragma unroll
            for (int p = 0; p + mm < 8; ++p) cacc[p + mm] = mfma16u(A2[p], G2[mm], cacc[p + mm]);
        }
    }
    f32x4 keep[2];
    {
        LAS f32x4* red = (LAS f32x4*)(smem + X_CRED) + w * 512;
#pragma unroll
        for (int j = 0; j < 4; ++j) {
            if (qp != j) { red[(2 * j) * 64 + lane] = cacc[2 * j]; red[(2 * j + 1) * 64 + lane] = cacc[2 * j + 1]; }
            else { keep[0] = cacc[2 * j]; keep[1] = cacc[2 * j + 1]; }
        }
    }
    BAR();
    STAMP(11);
    {
        const LAS f32x4* red = (const LAS f32x4*)(smem + X_CRED) + cw * 2048;
        const int ap = lane & 15, g = lane >> 4;
        const LAS char* xrl = smem + cw * CH_BYTES + 8 * HC_STRIDE;
#pragma unroll
        for (int e2 = 0; e2 < 2; ++e2) {
            const int c = 2 * qp + e2;
            f32x4 s = keep[e2];
#pragma unroll
            for (int j = 1; j < 4; ++j) s += red[((qp + j) & 3) * 512 + c * 64 + lane];
            const int s0 = 256 * c + 16 * ap + 4 * g;
            const u32x2 kx = *(const LAS u32x2*)(xrl + (2044 - s0) * 2);
            const float kv0 = bfhi(kx[1]), kv1 = bflo(kx[1]), kv2 = bfhi(kx[0]), kv3 = bflo(kx[0]);
            const float y0 = fmaf(kv0, Dval, s[0]), y1 = fmaf(kv1, Dval, s[1]);
            const float y2 = fmaf(kv2, Dval, s[2]), y3 = fmaf(kv3, Dval, s[3]);
            u32x2 o = {pk2(y0, y1), pk2(y2, y3)};
            __builtin_amdgcn_raw_buffer_store_b64(o, rs, WS_YT + ((unsigned)(2 * bid + cw) * S + s0) * 2, 0, SC1);
        }
    }
    if (lane == 0) poll_ge(cnt_ptr(a.ws, C_H + (rt >> 1)), 14u);
    WAIT_V(0);
    {
        unsigned old = 0u;
        if (lane == 0) old = __hip_atomic_fetch_add((LAS unsigned*)(smem + X_HC + 4), 1u, __ATOMIC_RELAXED, __HIP_MEMORY_SCOPE_WORKGROUP);
        if (lane == 0 && old == 7u) (void)__hip_atomic_fetch_add(cnt_ptr(a.ws, C_Y + ((2 * bid) >> 6)), 1u, RLX);
    }
    STAMP(12);
    u32x4 hw[HWIN][2];
    auto hload = [&](int i) {
        hw[i % HWIN][0] = __builtin_amdgcn_raw_buffer_load_b128(rs, hto, kso(i), LD_AUX);
        hw[i % HWIN][1] = __builtin_amdgcn_raw_buffer_load_b128(rs, hto + 112 * 1024, kso(i), LD_AUX);
    };
#pragma unroll
    for (int i = 0; i < HWIN; ++i) hload(i);
    const int tp = tid >> 4, cq = tid & 15;
    const int tok0 = rt * 64 + 2 * tp, d0 = ctile * 64 + 4 * cq;
    u32x2 q0, q1; u32x4 stu;
    SCHED();
    BAR();
    f32x16 acc2[2][2];
#pragma unroll
    for (int i = 0; i < 2; ++i)
#pragma unroll
        for (int j = 0; j < 2; ++j)
#pragma unroll
            for (int e = 0; e < 16; ++e) acc2[i][j][e] = 0.f;
    unsigned yv[4];
#pragma unroll
    for (int i = 0; i < 14; ++i) {
        const u32x4 hf0 = hw[i % HWIN][0], hf1 = hw[i % HWIN][1];
        if (i + HWIN < 14) hload(i + HWIN);
        if (i == 9) {
            if (lane == 0) poll_ge(cnt_ptr(a.ws, C_Y + ctile), 32u);
            __builtin_amdgcn_fence(__ATOMIC_ACQUIRE, "wavefront");
#pragma unroll
            for (int j = 0; j < 4; ++j)
                yv[j] = __builtin_amdgcn_raw_buffer_load_b32(rs, WS_YT + (unsigned)(d0 + j) * 4096 + tok0 * 2, 0, SC1);
            q0 = *(const u32x2*)(a.q + (size_t)tok0 * D + d0);
            q1 = *(const u32x2*)(a.q + (size_t)(tok0 + 1) * D + d0);
            stu = __builtin_amdgcn_raw_buffer_load_b128(rs, WS_STATS + (unsigned)tok0 * 8, 0, SC1);
        }
        const u32x4 wf0 = wd[i % WWIN][0], wf1 = wd[i % WWIN][1];
        acc2[0][0] = mfma32u(wf0, hf0, acc2[0][0]);
        acc2[0][1] = mfma32u(wf0, hf1, acc2[0][1]);
        acc2[1][0] = mfma32u(wf1, hf0, acc2[1][0]);
        acc2[1][1] = mfma32u(wf1, hf1, acc2[1][1]);
        SCHED();
    }
    STAMP(13);
    {
        LAS char* rbp = smem + w * (64 * RED_STRIDE);
#pragma unroll
        for (int i = 0; i < 2; ++i)
#pragma unroll
            for (int j = 0; j < 2; ++j)
#pragma unroll
                for (int g = 0; g < 4; ++g) {
                    f32x4 v = {acc2[i][j][4 * g], acc2[i][j][4 * g + 1], acc2[i][j][4 * g + 2], acc2[i][j][4 * g + 3]};
                    *(LAS f32x4*)(rbp + (j * 32 + r) * RED_STRIDE + (i * 32 + 8 * g + 4 * half) * 4) = v;
                }
    }
    float xv[2][4], sv[2][4], r2[2];
#pragma unroll
    for (int e = 0; e < 2; ++e) {
        const float mb = __uint_as_float(stu[2 * e]), rd = __uint_as_float(stu[2 * e + 1]);
        const u32x2 qq = e ? q1 : q0;
        r2[e] = rd * rd;
#pragma unroll
        for (int j = 0; j < 4; ++j) {
            const float x = (j & 1) ? bfhi(qq[j >> 1]) : bflo(qq[j >> 1]);
            const float y = e ? bfhi(yv[j]) : bflo(yv[j]);
            xv[e][j] = x; sv[e][j] = (x - mb) * rd * y;
            asm volatile("" : "+v"(xv[e][j]), "+v"(sv[e][j]));
        }
    }
#pragma nounroll
    for (int pass = (DRY && w < 4) ? 0 : 1; pass < 2; ++pass) {
        if (pass == 1) BAR();
        f32x4 o[2];
#pragma unroll
        for (int e = 0; e < 2; ++e) {
            const LAS char* rbp = smem + (2 * tp + e) * RED_STRIDE + cq * 16;
            f32x4 s0 = *(const LAS f32x4*)rbp, s1 = *(const LAS f32x4*)(rbp + 64 * RED_STRIDE);
#pragma unroll
            for (int ww = 2; ww < 8; ww += 2) { s0 += *(const LAS f32x4*)(rbp + ww * (64 * RED_STRIDE)); s1 += *(const LAS f32x4*)(rbp + (ww + 1) * (64 * RED_STRIDE)); }
            o[e] = s0 + s1;
        }
        u32x2 ov2[2];
#pragma unroll
        for (int e = 0; e < 2; ++e) {
            float res[4];
#pragma unroll
            for (int j = 0; j < 4; ++j) {
                const float ov = o[e][j] * r2[e];
                const float gg = ov * __builtin_amdgcn_rcpf(1.0f + __expf(-ov));
                res[j] = fmaf(sv[e][j], gg, xv[e][j]);
            }
            ov2[e] = u32x2{pk2(res[0], res[1]), pk2(res[2], res[3])};
            asm volatile("" : "+v"(ov2[e]));
        }
        if (pass == 1) {
#pragma unroll
            for (int e = 0; e < 2; ++e)
                __builtin_amdgcn_raw_buffer_store_b64(ov2[e], mkrs(a.out, S * D * 2), (unsigned)((tok0 + e) * D + d0) * 2u, 0, SC1);
        }
    }
    STAMP(15);
    STAMP_FLUSH();
}

extern "C" void kernel_launch(void* const* d_in, const int* in_sizes, int n_in,
                              void* d_out, int out_size, void* d_ws, size_t ws_size,
                              hipStream_t stream) {
    (void)hipFuncSetAttribute((const void*)fused_kernel, hipFuncAttributeMaxDynamicSharedMemorySize, LDS_BYTES);
    Args a;
    a.q = (const u16*)d_in[0]; a.k = (const u16*)d_in[1]; a.v = (const u16*)d_in[2]; a.sk = (const u16*)d_in[3];
    a.ssm = (const u16*)d_in[4]; a.Dv = (const u16*)d_in[5];
    a.wg = (const u16*)d_in[7]; a.wu = (const u16*)d_in[8]; a.wd = (const u16*)d_in[9];
    a.ws = (char*)d_ws; a.out = (u16*)d_out;
    fused_kernel<<<NMAIN + NPREP + 1, 512, LDS_BYTES, stream>>>(a);
}
```

```cpp
#include <hip/hip_runtime.h>
#include <stdint.h>

typedef unsigned short u16;
typedef __attribute__((ext_vector_type(4))) short s16x4;
typedef __attribute__((ext_vector_type(8))) short s16x8;
typedef __attribute__((ext_vector_type(8))) __bf16 bf16x8;
typedef __attribute__((ext_vector_type(2))) __bf16 bf16x2;
typedef __attribute__((ext_vector_type(16))) float f32x16;
typedef __attribute__((ext_vector_type(4))) float f32x4;
typedef __attribute__((ext_vector_type(4))) unsigned u32x4;
typedef __attribute__((ext_vector_type(2))) unsigned u32x2;
#define GAS __attribute__((address_space(1)))
#define LAS __attribute__((address_space(3)))
#define DI __device__ __forceinline__
typedef GAS unsigned gu32;
typedef GAS unsigned long long gu64;
typedef unsigned long long u64;
#define RLX __ATOMIC_RELAXED, __HIP_MEMORY_SCOPE_AGENT

static constexpr int S = 2048, D = 448, F = 1792;
static constexpr int NMAIN = 224, NPREP = 28;
static constexpr unsigned SY_M = 0, SY_A = 2048, SY_CNT = 4096;
static constexpr int C_KV = 0, C_ST = 7, C_HS = 21, C_WD = 22, C_H = 29, C_Y = 45, C_CS = 52, C_N = 59;
static constexpr unsigned WS_STATS = 16384;
static constexpr unsigned WS_CS    = WS_STATS + S * 8;
static constexpr unsigned WS_HT    = WS_CS + 16384;
static constexpr unsigned WS_WDT   = WS_HT + S * F * 2;
static constexpr unsigned WS_KVR   = WS_WDT + F * D * 2;
static constexpr unsigned WS_YT    = WS_KVR + S * D * 2;
static constexpr unsigned WS_END   = WS_YT + S * D * 2;
static constexpr int SC1 = 16;
static constexpr int PRE = 8;
static constexpr int HDELAY = 3;
static constexpr int LD_AUX = SC1;
static constexpr unsigned SPIN_CAP = 1u << 16;

DI float bflo(unsigned u) { return __uint_as_float(u << 16); }
DI float bfhi(unsigned u) { return __uint_as_float(u & 0xffff0000u); }
DI unsigned pk2(float a, float b) { bf16x2 r = {(__bf16)a, (__bf16)b}; return __builtin_bit_cast(unsigned, r); }
DI float rbf(float a) { return bflo(pk2(a, a)); }
template <int CTRL> DI float dpp_add(float v) {
    return v + __int_as_float(__builtin_amdgcn_update_dpp(0, __float_as_int(v), CTRL, 0xf, 0xf, true));
}
DI float sum8(float v) { v = dpp_add<0xB1>(v); v = dpp_add<0x4E>(v); return dpp_add<0x141>(v); }
DI float xor16_add(float v) { auto r = __builtin_amdgcn_permlane16_swap(__float_as_uint(v), __float_as_uint(v), false, false); return __uint_as_float(r[0]) + __uint_as_float(r[1]); }
DI float xor32_add(float v) { auto r = __builtin_amdgcn_permlane32_swap(__float_as_uint(v), __float_as_uint(v), false, false); return __uint_as_float(r[0]) + __uint_as_float(r[1]); }
DI float sum32(float v) { v = sum8(v); v = dpp_add<0x140>(v); return xor16_add(v); }
DI f32x16 mfma32(s16x8 a, s16x8 b, f32x16 c) {
    return __builtin_amdgcn_mfma_f32_32x32x16_bf16(__builtin_bit_cast(bf16x8, a), __builtin_bit_cast(bf16x8, b), c, 0, 0, 0);
}
DI f32x4 mfma16u(u32x4 a, u32x4 b, f32x4 c) {
    return __builtin_amdgcn_mfma_f32_16x16x32_bf16(__builtin_bit_cast(bf16x8, a), __builtin_bit_cast(bf16x8, b), c, 0, 0, 0);
}
DI f32x16 mfma32u(u32x4 a, u32x4 b, f32x16 c) { return mfma32(__builtin_bit_cast(s16x8, a), __builtin_bit_cast(s16x8, b), c); }
#define WAIT_V(n) asm volatile("s_waitcnt vmcnt(" #n ")" ::: "memory")
#define BAR() do { asm volatile("s_waitcnt lgkmcnt(0)" ::: "memory"); __builtin_amdgcn_s_barrier(); asm volatile("" ::: "memory"); } while (0)
#define SCHED() __builtin_amdgcn_sched_barrier(0)
DI __amdgpu_buffer_rsrc_t mkrs(const void* p, unsigned bytes) { return __builtin_amdgcn_make_buffer_rsrc((void*)p, 0, bytes, 0x00020000); }
DI gu32* cnt_ptr(char* ws, int i) { return (gu32*)(ws + SY_CNT + 128 * i); }
DI void poll_eq(gu32* p, unsigned want) {
    for (unsigned sp = 0; sp < SPIN_CAP; ++sp) { if (__hip_atomic_load(p, RLX) == want) break; __builtin_amdgcn_s_sleep(1); }
}
DI void poll_eq64(gu64* p, u64 want) {
    for (unsigned sp = 0; sp < SPIN_CAP; ++sp) { if (__hip_atomic_load(p, RLX) == want) break; __builtin_amdgcn_s_sleep(1); }
}
DI void poll_ge(gu32* p, unsigned want) {
    for (unsigned sp = 0; sp < SPIN_CAP; ++sp) { if (__hip_atomic_load(p, RLX) >= want) break; __builtin_amdgcn_s_sleep(1); }
}

struct Args {
    const u16* q; const u16* k; const u16* v; const u16* sk; const u16* ssm; const u16* Dv;
    const u16* wg; const u16* wu; const u16* wd;
    char* ws; u16* out;
};

static constexpr int G1_STAGE = 49152;
static constexpr int X_CS = 3 * G1_STAGE;
static constexpr int HC_STRIDE = 4160;
static constexpr int CH_BYTES = 8 * HC_STRIDE + 4608;
static constexpr int X_CRED = 2 * CH_BYTES;
static constexpr int RED_STRIDE = 272;
static constexpr int X_HC = X_CS + 1024;
static constexpr int X_SC = X_HC + 16;
static constexpr int LDS_BYTES = X_SC + 8192;
static constexpr int WWIN = 14;
static constexpr int HWIN = 6;

DI void dma16(const GAS char* sbase, unsigned voff, unsigned ldst) {
    unsigned keep;
    asm volatile("s_mov_b32 %0, m0\n\ts_mov_b32 m0, %3\n\ts_nop 0\n\tglobal_load_lds_dwordx4 %1, %2\n\ts_mov_b32 m0, %0"
                 : "=&s"(keep) : "v"(voff), "s"(sbase), "s"(ldst));
}


DI void service_block(const Args& a, LAS char* smem) {
    const int lane = threadIdx.x & 63, w = threadIdx.x >> 6;
    const __amdgpu_buffer_rsrc_t rs = mkrs(a.ws, WS_END);
    volatile LAS unsigned* stop = (volatile LAS unsigned*)smem;
    if (w == 0) {
        if (lane < C_N) __hip_atomic_store(cnt_ptr(a.ws, lane), 0u, RLX);
        if (lane == 0) *stop = 0u;
        WAIT_V(0);
    }
    BAR();
    if (w == 0) {
        for (unsigned it = 0; it < SPIN_CAP; ++it) {
            const unsigned c = __hip_atomic_load(cnt_ptr(a.ws, lane < C_N ? lane : 0), RLX);
            const bool ok = (lane >= C_CS && lane < C_N) ? (c >= 4u) : (lane < C_ST) ? (c >= 32u) : true;
            if (__all(ok)) break;
            __builtin_amdgcn_s_sleep(8);
        }
        if (lane == 0) *stop = 1u;
    } else if (w < 3) {
        for (int i = 0; i < (w - 1) * 4; ++i) __builtin_amdgcn_s_sleep(2);
        for (unsigned it = 0; it < SPIN_CAP; ++it) {
            const u32x4 m0 = __builtin_amdgcn_raw_buffer_load_b128(rs, SY_M + lane * 32, 0, SC1);
            const u32x4 m1 = __builtin_amdgcn_raw_buffer_load_b128(rs, SY_M + lane * 32 + 16, 0, SC1);
            __builtin_amdgcn_raw_buffer_store_b128(m0, rs, SY_A + lane * 32, 0, SC1);
            __builtin_amdgcn_raw_buffer_store_b128(m1, rs, SY_A + lane * 32 + 16, 0, SC1);
            if (*stop) break;
            __builtin_amdgcn_s_sleep(6);
        }
    }
}

DI u32x2 row_stats(const u32x4 (&xs)[7]) {
    float s0 = 0.f, s1 = 0.f;
#pragma unroll
    for (int i = 0; i < 7; ++i)
#pragma unroll
        for (int j = 0; j < 4; ++j) { s0 += bflo(xs[i][j]); s1 += bfhi(xs[i][j]); }
    float sum = s0 + s1;
    sum = sum8(sum);
    const float m = sum * (1.0f / D);
    float q0 = 0.f, q1 = 0.f;
#pragma unroll
    for (int i = 0; i < 7; ++i)
#pragma unroll
        for (int j = 0; j < 4; ++j) {
            const float d0 = bflo(xs[i][j]) - m, d1 = bfhi(xs[i][j]) - m;
            q0 = fmaf(d0, d0, q0); q1 = fmaf(d1, d1, q1);
        }
    float sq = q0 + q1;
    sq = sum8(sq);
    const float var = sq * (1.0f / D);
    const float mb = rbf(m), vb = rbf(var);
    const float den = rbf(sqrtf(rbf(vb + 1e-5f)));
    return u32x2{__float_as_uint(mb), __float_as_uint(1.0f / den)};
}

DI void prep_block(const Args& a, LAS char* smem) {
    const int tid = threadIdx.x, lane = tid & 63;
    const int w = __builtin_amdgcn_readfirstlane(tid >> 6);
    const int g = (blockIdx.x - NMAIN) * 8 + w;
    const __amdgpu_buffer_rsrc_t rs = mkrs(a.ws, WS_END);
    gu64* mbA = (gu64*)(a.ws + SY_A) + blockIdx.x;
    const u64 chal = __builtin_amdgcn_s_memrealtime();
    if (tid == 0) __hip_atomic_store((gu64*)(a.ws + SY_M) + blockIdx.x, chal, RLX);
    for (int i = 0; i < HDELAY; ++i) __builtin_amdgcn_s_sleep(16);
    const int hb = blockIdx.x - NMAIN;
    const u16* wsrc = (hb < 14 ? a.wg : a.wu) + (hb < 14 ? hb : hb - 14) * 128 + (tid & 15) * 8;
    u32x4 wr[14];
#pragma unroll
    for (int i = 0; i < 14; ++i) wr[i] = *(const u32x4*)(wsrc + (size_t)((tid >> 4) * 14 + i) * F);
    u32x4 xs[7];
    if (g < 32) {
        const int srow = (224 + g) * 8 + (lane >> 3);
#pragma unroll
        for (int i = 0; i < 7; ++i) xs[i] = *(const u32x4*)(a.q + (size_t)srow * D + ((lane & 7) + 8 * i) * 8);
    }
    SCHED();
    float cs[8];
#pragma unroll
    for (int j = 0; j < 8; ++j) cs[j] = 0.f;
#pragma unroll
    for (int i = 0; i < 14; ++i)
#pragma unroll
        for (int e = 0; e < 4; ++e) { cs[2 * e] += bflo(wr[i][e]); cs[2 * e + 1] += bfhi(wr[i][e]); }
#pragma unroll
    for (int j = 0; j < 8; ++j) cs[j] = xor32_add(xor16_add(cs[j]));
    LAS f32x4* red = (LAS f32x4*)smem;
    if (lane < 16) { red[(w * 16 + lane) * 2] = f32x4{cs[0], cs[1], cs[2], cs[3]}; red[(w * 16 + lane) * 2 + 1] = f32x4{cs[4], cs[5], cs[6], cs[7]}; }
    if (g < 32) {
        const u32x2 o = row_stats(xs);
        if ((lane & 7) == 0) __builtin_amdgcn_raw_buffer_store_b64(o, rs, WS_STATS + (unsigned)((224 + g) * 8 + (lane >> 3)) * 8, 0, SC1);
    }
    BAR();
    if (tid < 32) {
        f32x4 s = red[tid];
#pragma unroll
        for (int ww = 1; ww < 8; ++ww) s += red[ww * 32 + tid];
        __builtin_amdgcn_raw_buffer_store_b128(__builtin_bit_cast(u32x4, s), rs, WS_CS + (unsigned)((hb < 14 ? 0 : 1792) + (hb < 14 ? hb : hb - 14) * 128 + tid * 4) * 4, 0, SC1);
    }
    WAIT_V(0);
    BAR();
    if (w == 0) {
        if (lane == 0) poll_eq64(mbA, chal);
        const int cbh = hb < 14 ? hb : hb - 14;
        if (lane == 0 || (lane == 1 && hb < 4)) (void)__hip_atomic_fetch_add(cnt_ptr(a.ws, lane == 0 ? C_CS + (cbh >> 1) : C_HS), 1u, RLX);
    }
}

__global__ __launch_bounds__(512, 2) void fused_kernel(Args a) {
    extern __shared__ __attribute__((aligned(16))) char smem_[];
    LAS char* smem = (LAS char*)smem_;
    const int bid = blockIdx.x;
    if (bid >= NMAIN) {
        if (bid == NMAIN + NPREP) service_block(a, smem); else prep_block(a, smem);
        return;
    }
    const int tid = threadIdx.x, lane = tid & 63;
    const int w = __builtin_amdgcn_readfirstlane(tid >> 6);
    const int xcd = bid & 7, idx = bid >> 3;
    const __amdgpu_buffer_rsrc_t rs = mkrs(a.ws, WS_END);
    const int rb = (xcd >> 1) * 4 + (idx & 3);
    const int cb = (xcd & 1) * 7 + (idx >> 2);
    const int rt = xcd * 4 + (idx & 3);
    const int ctile = idx >> 2;
    const int r = lane & 31, half = lane >> 5;
    const int rot = (2 * ctile + 3 * (idx & 3) + (idx & 2) / 2) % 14;
    const unsigned wdo = WS_WDT + (((unsigned)(ctile * 2) * 112 + 14 * w) * 64 + lane) * 16;
    const unsigned hto = WS_HT + (((unsigned)(rt * 2) * 112 + 14 * w) * 64 + lane) * 16;
    auto kso = [&](int i) { const int k = i + rot; return (unsigned)(k >= 14 ? k - 14 : k) * 1024u; };
    u32x4 wd[WWIN][2];
    auto wdload = [&](int n) {
        wd[(n >> 1) % WWIN][n & 1] = __builtin_amdgcn_raw_buffer_load_b128(rs, wdo + (n & 1) * 112 * 1024, kso(n >> 1), LD_AUX);
    };
    if (w >= 4) {
        const int lw = w - 4, lt = tid - 256;
        u32x4 xr[2], h0[2], h1[2];
        bool wd_early = false;
        auto taps = [&]() {
#pragma unroll
            for (int c2 = 0; c2 < 2; ++c2) {
                const u16* hs = a.ssm + (size_t)(2 * bid + c2) * S;
                h0[c2] = *(const u32x4*)(hs + (lt > 0 ? lt - 1 : 0) * 8);
                h1[c2] = *(const u32x4*)(hs + lt * 8);
            }
        };
        if (lw < 3) {
            const GAS char* gsrc; unsigned vo[2]; size_t cstep, istep; unsigned ldst;
            if (lw < 2) {
                gsrc = (const GAS char*)(lw ? a.wu : a.wg) + (size_t)cb * 256;
                vo[0] = vo[1] = (lane >> 4) * (F * 2) + (((lane & 15) ^ (((lane >> 4) << 2) | (lw << 1))) * 16);
                cstep = (size_t)64 * (F * 2); istep = (size_t)8 * (F * 2);
                ldst = 16384 + lw * 16384;
            } else {
                gsrc = (const GAS char*)a.q + (size_t)rb * 128 * D * 2;
#pragma unroll
                for (int e = 0; e < 2; ++e) { const int row = 8 * e + (lane >> 3); vo[e] = row * (D * 2) + (((lane & 7) ^ ((row >> 1) & 7)) * 16); }
                cstep = 128; istep = (size_t)16 * (D * 2);
                ldst = 0;
            }
            if (lw < 2) vo[1] += 4 * (F * 2);
            const unsigned lds0 = (unsigned)(uintptr_t)smem + ldst;
            auto pieces = [&](int c, int i0, int i1) {
#pragma unroll
                for (int i = i0; i < i1; ++i)
                    dma16(gsrc + c * cstep + (i >> 1) * istep, vo[i & 1], lds0 + (c % 3) * G1_STAGE + i * 1024);
            };
            auto stage = [&](int c) { pieces(c, 0, 16); };
            stage(0);
            pieces(1, 0, PRE);
            asm volatile("s_waitcnt vmcnt(%0)" :: "n"(PRE) : "memory");
            BAR();
            pieces(1, PRE, 16);
#pragma unroll
            for (int c = 0; c < 6; ++c) {
                if (c + 2 <= 6) stage(c + 2);
                asm volatile("" ::: "memory");
                SCHED();
                if (c == 4) taps();
                if (c == 5) {
                    wd_early = __builtin_amdgcn_readfirstlane(*(const LAS unsigned*)(smem + X_HC + 8)) != 0;
                    WAIT_V(4);
                    if (wd_early) {
#pragma unroll
                        for (int n = 0; n < 10; ++n) wdload(n);
                    }
                }
                SCHED();
                if (c == 4) WAIT_V(20);
                else if (c == 5) {}
                else WAIT_V(16);
                BAR();
            }
        } else {
            gu64* mbA = (gu64*)(a.ws + SY_A) + bid;
            const u64 chal = __builtin_amdgcn_s_memrealtime();
            if (lane == 0) __hip_atomic_store((gu64*)(a.ws + SY_M) + bid, chal, RLX);
            u32x4 wt[7], xs[7];
#pragma unroll
            for (int e = 0; e < 7; ++e) {
                const int t = 7 * bid + e, dt = t / 112, ks = t % 112;
                wt[e] = *(const u32x4*)(a.wd + (size_t)(16 * ks + (lane >> 2)) * D + 32 * dt + (lane & 3) * 8);
            }
#pragma unroll
            for (int i = 0; i < 7; ++i) xs[i] = *(const u32x4*)(a.q + (size_t)(8 * bid + (lane >> 3)) * D + ((lane & 7) + 8 * i) * 8);
            if (lane == 0) { *(LAS unsigned*)(smem + X_HC) = 0u; *(LAS unsigned*)(smem + X_HC + 4) = 0u; }
            SCHED();
            BAR();
            u64 ans = __hip_atomic_load(mbA, RLX);
            {
                LAS char* sc = smem + X_SC;
                const int gi = lane >> 4, hf = gi >> 1, blk = gi & 1, q4 = (lane >> 2) & 3, p4 = lane & 3;
                const int ra = (4 * hf + q4) * 64 + (16 * blk + 4 * p4) * 2;
#pragma unroll
                for (int e = 0; e < 7; ++e) *(LAS u32x4*)(sc + e * 1024 + lane * 16) = wt[e];
                asm volatile("s_waitcnt lgkmcnt(0)" ::: "memory");
#pragma unroll
                for (int e = 0; e < 7; ++e) {
                    s16x4 lo = __builtin_amdgcn_ds_read_tr16_b64_v4i16((LAS s16x4*)(sc + e * 1024 + ra));
                    s16x4 hi = __builtin_amdgcn_ds_read_tr16_b64_v4i16((LAS s16x4*)(sc + e * 1024 + ra + 512));
                    s16x8 o = __builtin_shufflevector(lo, hi, 0, 1, 2, 3, 4, 5, 6, 7);
                    __builtin_amdgcn_raw_buffer_store_b128(__builtin_bit_cast(u32x4, o), rs, WS_WDT + (unsigned)(7 * bid + e) * 1024 + lane * 16, 0, SC1);
                }
            }
            BAR();
            if (ans != chal) { if (lane == 0) poll_eq64(mbA, chal); }
            if (lane == 0) (void)__hip_atomic_fetch_add(cnt_ptr(a.ws, C_KV + bid % 7), 1u, RLX);
            {
                const u32x2 o = row_stats(xs);
                if ((lane & 7) == 0) __builtin_amdgcn_raw_buffer_store_b64(o, rs, WS_STATS + (unsigned)(8 * bid + (lane >> 3)) * 8, 0, SC1);
            }
            BAR();
            WAIT_V(0);
            if (lane < 2) (void)__hip_atomic_fetch_add(cnt_ptr(a.ws, lane == 0 ? C_WD + (bid >> 5) : C_ST + (bid >> 4)), 1u, RLX);
            BAR();
            taps();
            BAR();
            const int ci = (lane == 1) ? (rb < 14 ? C_ST + rb : C_HS) : (lane == 2) ? C_WD + ctile : (lane == 3) ? C_CS + (cb >> 1) : C_KV + (bid >> 5);
            const unsigned want = (lane == 1) ? (rb < 14 ? 16u : 4u) : (lane == 3) ? 4u : 32u;
            unsigned cnt = __hip_atomic_load(cnt_ptr(a.ws, ci), RLX);
            wd_early = __builtin_amdgcn_readlane(cnt, 2) >= 32;
            if (lane == 0) *(LAS unsigned*)(smem + X_HC + 8) = wd_early ? 1u : 0u;
            BAR();
            {
                if (!__all(cnt >= want)) cnt = __hip_atomic_load(cnt_ptr(a.ws, ci), RLX);
                if (wd_early) {
#pragma unroll
                    for (int n = 0; n < 10; ++n) wdload(n);
                }
                for (unsigned sp = 0; sp < SPIN_CAP; ++sp) {
                    if (__all(cnt >= want)) break;
                    __builtin_amdgcn_s_sleep(1);
                    cnt = __hip_atomic_load(cnt_ptr(a.ws, ci), RLX);
                }
            }
            BAR();
        }
        const u32x4 cv = __builtin_amdgcn_raw_buffer_load_b128(rs, WS_CS + (unsigned)((lane >> 5) * 1792 + cb * 128 + (lane & 31) * 4) * 4, 0, SC1);
#pragma unroll
        for (int c2 = 0; c2 < 2; ++c2)
            xr[c2] = __builtin_amdgcn_raw_buffer_load_b128(rs, WS_KVR + ((unsigned)(2 * bid + c2) * S + lt * 8) * 2, 0, SC1);
        if (!wd_early) {
#pragma unroll
            for (int n = 0; n < 10; ++n) wdload(n);
        }
#pragma unroll
        for (int n = 10; n < 28; ++n) wdload(n);
        if (lw == 0) *(LAS u32x4*)(smem + X_CS + lane * 16) = cv;
        BAR();
#pragma unroll
        for (int c2 = 0; c2 < 2; ++c2) {
            LAS char* cbp = smem + c2 * CH_BYTES;
            u32x4 hh0 = h0[c2];
            if (lt == 0) hh0 = u32x4{0, 0, 0, 0};
            const unsigned d[8] = {hh0[0], hh0[1], hh0[2], hh0[3], h1[c2][0], h1[c2][1], h1[c2][2], h1[c2][3]};
            unsigned al[7];
#pragma unroll
            for (int i = 0; i < 7; ++i) al[i] = __builtin_amdgcn_alignbit(d[i + 1], d[i], 16);
            const int t = lt + 1;
#pragma unroll
            for (int sg = 0; sg < 8; ++sg) {
                const int sh = sg + 1;
                u32x4 o;
#pragma unroll
                for (int e = 0; e < 4; ++e) o[e] = (sh & 1) ? al[(sh >> 1) + e] : d[(sh >> 1) + e];
                *(LAS u32x4*)(cbp + sg * HC_STRIDE + t * 16) = o;
            }
            if (lt < 8) *(LAS u32x4*)(cbp + lt * HC_STRIDE) = u32x4{0, 0, 0, 0};
            *(LAS u32x4*)(cbp + 8 * HC_STRIDE + lt * 16) = xr[c2];
            if (lt < 32) *(LAS u32x4*)(cbp + 8 * HC_STRIDE + 4096 + lt * 16) = u32x4{0, 0, 0, 0};
        }
    } else {
        const int s0 = (bid / 7) * 64 + (2 * w + half) * 8;
        const int dch = (bid % 7) * 64 + 2 * r;
        unsigned kr[11], vr[8];
#pragma unroll
        for (int i = 0; i < 11; ++i) {
            const int s = s0 - 3 + i;
            asm volatile("global_load_dword %0, %1, %2" : "=v"(kr[i]) : "v"((unsigned)((s < 0 ? 0 : s) * D + dch) * 2u), "s"((const GAS char*)a.k));
        }
#pragma unroll
        for (int i = 0; i < 8; ++i)
            asm volatile("global_load_dword %0, %1, %2" : "=v"(vr[i]) : "v"((unsigned)((s0 + i) * D + dch) * 2u), "s"((const GAS char*)a.v));
        u32x4 sk;
        asm volatile("global_load_dwordx4 %0, %1, %2" : "=v"(sk) : "v"((unsigned)dch * 8u), "s"((const GAS char*)a.sk));
        float ko[8];
        unsigned kpk[4];
        auto kv_part = [&](int c, int hp) {
            const float w0 = bflo(sk[2 * c]), w1 = bfhi(sk[2 * c]), w2 = bflo(sk[2 * c + 1]), w3 = bfhi(sk[2 * c + 1]);
            float kf[7];
#pragma unroll
            for (int i = 0; i < 7; ++i) {
                unsigned kk = kr[4 * hp + i];
                if (hp == 0 && i < 3) kk = (s0 == 0) ? 0u : kk;
                kf[i] = c ? bfhi(kk) : bflo(kk);
            }
#pragma unroll
            for (int i = 0; i < 4; ++i) {
                float acc = kf[i] * w3;
                acc = fmaf(kf[i + 1], w2, acc);
                acc = fmaf(kf[i + 2], w1, acc);
                acc = fmaf(kf[i + 3], w0, acc);
                ko[4 * hp + i] = acc * (c ? bfhi(vr[4 * hp + i]) : bflo(vr[4 * hp + i]));
            }
            kpk[3 - 2 * hp] = pk2(ko[4 * hp + 1], ko[4 * hp + 0]);
            kpk[2 - 2 * hp] = pk2(ko[4 * hp + 3], ko[4 * hp + 2]);
            if (hp == 1)
                __builtin_amdgcn_raw_buffer_store_b128(u32x4{kpk[0], kpk[1], kpk[2], kpk[3]}, rs, WS_KVR + ((unsigned)(dch + c) * S + (2040 - s0)) * 2, 0, SC1);
        };
        const int wm = w >> 1, wn = w & 1;
        unsigned xa[4], wa[4];
        {
            const int gi = lane >> 4, blk = gi & 1, q4 = (lane >> 2) & 3, p4 = lane & 3, gu = p4 >> 1;
#pragma unroll
            for (int t = 0; t < 4; ++t) xa[t] = (unsigned)(wm * 64 + r) * 128 + (((2 * t + half) ^ ((r >> 1) & 7)) * 16);
#pragma unroll
            for (int i = 0; i < 4; ++i) {
                const int pc = 2 * (4 * wn + i) + blk;
                wa[i] = 16384 + gu * 16384 + (8 * half + q4) * 256 + ((pc ^ ((q4 << 2) | (gu << 1))) * 16) + (p4 & 1) * 8;
            }
        }
        f32x16 acc[4][2];
#pragma unroll
        for (int i = 0; i < 4; ++i)
#pragma unroll
            for (int j = 0; j < 2; ++j)
#pragma unroll
                for (int e = 0; e < 16; ++e) acc[i][j][e] = 0.f;
        s16x8 xf[2][2], wf[2][4];
        auto rdfrag = [&](int ks) {
            const int c = ks >> 2, t = ks & 3, b = ks & 1;
            LAS char* sb = smem + (c % 3) * G1_STAGE;
#pragma unroll
            for (int j = 0; j < 2; ++j) xf[b][j] = *(const LAS s16x8*)(sb + xa[t] + j * 4096);
#pragma unroll
            for (int i = 0; i < 4; ++i) {
                s16x4 lo = __builtin_amdgcn_ds_read_tr16_b64_v4i16((LAS s16x4*)(sb + wa[i] + t * 4096));
                s16x4 hi = __builtin_amdgcn_ds_read_tr16_b64_v4i16((LAS s16x4*)(sb + wa[i] + t * 4096 + 1024));
                wf[b][i] = __builtin_shufflevector(lo, hi, 0, 1, 2, 3, 4, 5, 6, 7);
            }
        };
        float mean[2];
        asm volatile("s_waitcnt vmcnt(0)" : "+v"(kr[0]), "+v"(kr[1]), "+v"(kr[2]), "+v"(kr[3]), "+v"(kr[4]), "+v"(kr[5]), "+v"(kr[6]), "+v"(kr[7]), "+v"(kr[8]), "+v"(kr[9]), "+v"(kr[10]),
                     "+v"(vr[0]), "+v"(vr[1]), "+v"(vr[2]), "+v"(vr[3]), "+v"(vr[4]), "+v"(vr[5]), "+v"(vr[6]), "+v"(vr[7]), "+v"(sk));
        kv_part(0, 0); kv_part(0, 1); kv_part(1, 0); kv_part(1, 1);
        BAR();
        rdfrag(0);
#pragma unroll
        for (int ks = 0; ks < 28; ++ks) {
            const int c = ks >> 2, t = ks & 3, b = ks & 1;
            if (t == 3 && c < 6) {
                if (c == 0) WAIT_V(0);
                BAR();
                if (c == 5) {
#pragma unroll
                    for (int j = 0; j < 2; ++j)
                        mean[j] = __uint_as_float(__builtin_amdgcn_raw_buffer_load_b32(rs, WS_STATS + (unsigned)(rb * 128 + (2 * wm + j) * 32 + r) * 8, 0, SC1));
                }
            }
            if (ks < 27) rdfrag(ks + 1);
#pragma unroll
            for (int i = 0; i < 4; ++i) {
                acc[i][0] = mfma32(wf[b][i], xf[b][0], acc[i][0]);
                acc[i][1] = mfma32(wf[b][i], xf[b][1], acc[i][1]);
            }
#pragma unroll
            for (int i = 0; i < 8; ++i) {
                __builtin_amdgcn_sched_group_barrier(0x008, 1, 0);
                if (i < 5) __builtin_amdgcn_sched_group_barrier(0x100, 2, 0);
            }
            SCHED();
        }
        BAR();
        const LAS float* cs = (const LAS float*)(smem + X_CS);
        f32x4 cva[4][4];
#pragma unroll
        for (int i = 0; i < 4; ++i)
#pragma unroll
            for (int g = 0; g < 4; ++g) cva[i][g] = *(const LAS f32x4*)(cs + (g & 1) * 128 + 16 * (4 * wn + i) + 8 * (g >> 1) + 4 * half);
        SCHED();
#pragma unroll
        for (int i = 0; i < 4; ++i) {
            const int ct = 4 * wn + i;
            const f32x4 (&cv)[4] = cva[i];
#pragma unroll
            for (int j = 0; j < 2; ++j) {
                float hv[8];
#pragma unroll
                for (int e = 0; e < 4; ++e) {
                    float g0 = acc[i][j][e] - mean[j] * cv[0][e];
                    float u0 = acc[i][j][4 + e] - mean[j] * cv[1][e];
                    float g1 = acc[i][j][8 + e] - mean[j] * cv[2][e];
                    float u1 = acc[i][j][12 + e] - mean[j] * cv[3][e];
                    hv[e] = fmaxf(g0, 0.f) * u0;
                    hv[4 + e] = fmaxf(g1, 0.f) * u1;
                }
                u32x4 o = {pk2(hv[0], hv[1]), pk2(hv[2], hv[3]), pk2(hv[4], hv[5]), pk2(hv[6], hv[7])};
                const int T = rb * 4 + 2 * wm + j, KS = cb * 8 + ct;
                __builtin_amdgcn_raw_buffer_store_b128(o, rs, WS_HT + (((unsigned)T * 112 + KS) * 64 + lane) * 16, 0, SC1);
                wdload(2 * i + j);
                SCHED();
            }
        }
    }
    const float Dval = bflo((unsigned)a.Dv[2 * bid + (w >> 2)]);
    BAR();
    const int cw = w >> 2, qp = w & 3;
    f32x4 cacc[8];
#pragma unroll
    for (int c = 0; c < 8; ++c) cacc[c] = f32x4{0.f, 0.f, 0.f, 0.f};
    {
        LAS char* cbp = smem + cw * CH_BYTES;
        const int m = lane & 15, g = lane >> 4;
        const LAS char* ab0 = cbp + (m & 7) * HC_STRIDE + (8 * qp + g + (m >> 3)) * 16;
        const LAS char* gb0 = cbp + 8 * HC_STRIDE + (2032 - 256 * 7 + 64 * qp - 16 * m + 8 * g) * 2;
        u32x4 A[8], G[8], A2[8], G2[8];
#pragma unroll
        for (int p = 0; p < 8; ++p) {
            G[p] = *(const LAS u32x4*)(gb0 + (7 - p) * 512);
            A[p] = *(const LAS u32x4*)(ab0 + p * 512);
        }
        SCHED();
#pragma unroll
        for (int p = 0; p < 8; ++p) {
#pragma unroll
            for (int c = p; c < 8; ++c) cacc[c] = mfma16u(A[p], G[c - p], cacc[c]);
            A2[p] = *(const LAS u32x4*)(ab0 + 64 + p * 512);
            G2[7 - p] = *(const LAS u32x4*)(gb0 + 64 + p * 512);
            if (w < 4) { wdload(8 + 2 * p); wdload(9 + 2 * p); if (p < 4) wdload(24 + p); }
            if (p == 3 && w < 4) {
                WAIT_V(13);
                unsigned old = 0u;
                if (lane == 0) old = __hip_atomic_fetch_add((LAS unsigned*)(smem + X_HC), 1u, __ATOMIC_RELAXED, __HIP_MEMORY_SCOPE_WORKGROUP);
                if (lane == 0 && old == 3u) (void)__hip_atomic_fetch_add(cnt_ptr(a.ws, C_H + rb), 1u, RLX);
            }
            SCHED();
        }
#pragma unroll
        for (int mm = 7; mm >= 0; --mm) {
#pragma unroll
            for (int p = 0; p + mm < 8; ++p) cacc[p + mm] = mfma16u(A2[p], G2[mm], cacc[p + mm]);
        }
    }
    f32x4 keep[2];
    {
        LAS f32x4* red = (LAS f32x4*)(smem + X_CRED) + w * 512;
#pragma unroll
        for (int j = 0; j < 4; ++j) {
            if (qp != j) { red[(2 * j) * 64 + lane] = cacc[2 * j]; red[(2 * j + 1) * 64 + lane] = cacc[2 * j + 1]; }
            else { keep[0] = cacc[2 * j]; keep[1] = cacc[2 * j + 1]; }
        }
    }
    BAR();
    {
        const LAS f32x4* red = (const LAS f32x4*)(smem + X_CRED) + cw * 2048;
        const int ap = lane & 15, g = lane >> 4;
        const LAS char* xrl = smem + cw * CH_BYTES + 8 * HC_STRIDE;
#pragma unroll
        for (int e2 = 0; e2 < 2; ++e2) {
            const int c = 2 * qp + e2;
            f32x4 s = keep[e2];
#pragma unroll
            for (int j = 1; j < 4; ++j) s += red[((qp + j) & 3) * 512 + c * 64 + lane];
            const int s0 = 256 * c + 16 * ap + 4 * g;
            const u32x2 kx = *(const LAS u32x2*)(xrl + (2044 - s0) * 2);
            const float kv0 = bfhi(kx[1]), kv1 = bflo(kx[1]), kv2 = bfhi(kx[0]), kv3 = bflo(kx[0]);
            const float y0 = fmaf(kv0, Dval, s[0]), y1 = fmaf(kv1, Dval, s[1]);
            const float y2 = fmaf(kv2, Dval, s[2]), y3 = fmaf(kv3, Dval, s[3]);
            u32x2 o = {pk2(y0, y1), pk2(y2, y3)};
            __builtin_amdgcn_raw_buffer_store_b64(o, rs, WS_YT + ((unsigned)(2 * bid + cw) * S + s0) * 2, 0, SC1);
        }
    }
    if (lane == 0) poll_ge(cnt_ptr(a.ws, C_H + (rt >> 1)), 14u);
    WAIT_V(0);
    {
        unsigned old = 0u;
        if (lane == 0) old = __hip_atomic_fetch_add((LAS unsigned*)(smem + X_HC + 4), 1u, __ATOMIC_RELAXED, __HIP_MEMORY_SCOPE_WORKGROUP);
        if (lane == 0 && old == 7u) (void)__hip_atomic_fetch_add(cnt_ptr(a.ws, C_Y + ((2 * bid) >> 6)), 1u, RLX);
    }
    u32x4 hw[HWIN][2];
    auto hload = [&](int i) {
        hw[i % HWIN][0] = __builtin_amdgcn_raw_buffer_load_b128(rs, hto, kso(i), LD_AUX);
        hw[i % HWIN][1] = __builtin_amdgcn_raw_buffer_load_b128(rs, hto + 112 * 1024, kso(i), LD_AUX);
    };
#pragma unroll
    for (int i = 0; i < HWIN; ++i) hload(i);
    const int tp = tid >> 4, cq = tid & 15;
    const int tok0 = rt * 64 + 2 * tp, d0 = ctile * 64 + 4 * cq;
    u32x2 q0, q1; u32x4 stu;
    SCHED();
    BAR();
    f32x16 acc2[2][2];
#pragma unroll
    for (int i = 0; i < 2; ++i)
#pragma unroll
        for (int j = 0; j < 2; ++j)
#pragma unroll
            for (int e = 0; e < 16; ++e) acc2[i][j][e] = 0.f;
    unsigned yv[4];
#pragma unroll
    for (int i = 0; i < 14; ++i) {
        const u32x4 hf0 = hw[i % HWIN][0], hf1 = hw[i % HWIN][1];
        if (i + HWIN < 14) hload(i + HWIN);
        if (i == 9) {
            if (lane == 0) poll_ge(cnt_ptr(a.ws, C_Y + ctile), 32u);
            __builtin_amdgcn_fence(__ATOMIC_ACQUIRE, "wavefront");
#pragma unroll
            for (int j = 0; j < 4; ++j)
                yv[j] = __builtin_amdgcn_raw_buffer_load_b32(rs, WS_YT + (unsigned)(d0 + j) * 4096 + tok0 * 2, 0, SC1);
            q0 = *(const u32x2*)(a.q + (size_t)tok0 * D + d0);
            q1 = *(const u32x2*)(a.q + (size_t)(tok0 + 1) * D + d0);
            stu = __builtin_amdgcn_raw_buffer_load_b128(rs, WS_STATS + (unsigned)tok0 * 8, 0, SC1);
        }
        const u32x4 wf0 = wd[i % WWIN][0], wf1 = wd[i % WWIN][1];
        acc2[0][0] = mfma32u(wf0, hf0, acc2[0][0]);
        acc2[0][1] = mfma32u(wf0, hf1, acc2[0][1]);
        acc2[1][0] = mfma32u(wf1, hf0, acc2[1][0]);
        acc2[1][1] = mfma32u(wf1, hf1, acc2[1][1]);
        SCHED();
    }
    {
        LAS char* rbp = smem + w * (64 * RED_STRIDE);
#pragma unroll
        for (int i = 0; i < 2; ++i)
#pragma unroll
            for (int j = 0; j < 2; ++j)
#pragma unroll
                for (int g = 0; g < 4; ++g) {
                    f32x4 v = {acc2[i][j][4 * g], acc2[i][j][4 * g + 1], acc2[i][j][4 * g + 2], acc2[i][j][4 * g + 3]};
                    *(LAS f32x4*)(rbp + (j * 32 + r) * RED_STRIDE + (i * 32 + 8 * g + 4 * half) * 4) = v;
                }
    }
    float xv[2][4], sv[2][4], r2[2];
#pragma unroll
    for (int e = 0; e < 2; ++e) {
        const float mb = __uint_as_float(stu[2 * e]), rd = __uint_as_float(stu[2 * e + 1]);
        const u32x2 qq = e ? q1 : q0;
        r2[e] = rd * rd;
#pragma unroll
        for (int j = 0; j < 4; ++j) {
            const float x = (j & 1) ? bfhi(qq[j >> 1]) : bflo(qq[j >> 1]);
            const float y = e ? bfhi(yv[j]) : bflo(yv[j]);
            xv[e][j] = x; sv[e][j] = (x - mb) * rd * y;
            asm volatile("" : "+v"(xv[e][j]), "+v"(sv[e][j]));
        }
    }
    BAR();
    {
        f32x4 o[2];
#pragma unroll
        for (int e = 0; e < 2; ++e) {
            const LAS char* rbp = smem + (2 * tp + e) * RED_STRIDE + cq * 16;
            f32x4 s0 = *(const LAS f32x4*)rbp, s1 = *(const LAS f32x4*)(rbp + 64 * RED_STRIDE);
#pragma unroll
            for (int ww = 2; ww < 8; ww += 2) { s0 += *(const LAS f32x4*)(rbp + ww * (64 * RED_STRIDE)); s1 += *(const LAS f32x4*)(rbp + (ww + 1) * (64 * RED_STRIDE)); }
            o[e] = s0 + s1;
        }
#pragma unroll
        for (int e = 0; e < 2; ++e) {
            float res[4];
#pragma unroll
            for (int j = 0; j < 4; ++j) {
                const float ov = o[e][j] * r2[e];
                const float gg = ov * __builtin_amdgcn_rcpf(1.0f + __expf(-ov));
                res[j] = fmaf(sv[e][j], gg, xv[e][j]);
            }
            const u32x2 ov2 = {pk2(res[0], res[1]), pk2(res[2], res[3])};
            __builtin_amdgcn_raw_buffer_store_b64(ov2, mkrs(a.out, S * D * 2), (unsigned)((tok0 + e) * D + d0) * 2u, 0, SC1);
        }
    }
}

extern "C" void kernel_launch(void* const* d_in, const int* in_sizes, int n_in,
                              void* d_out, int out_size, void* d_ws, size_t ws_size,
                              hipStream_t stream) {
    (void)hipFuncSetAttribute((const void*)fused_kernel, hipFuncAttributeMaxDynamicSharedMemorySize, LDS_BYTES);
    Args a;
    a.q = (const u16*)d_in[0]; a.k = (const u16*)d_in[1]; a.v = (const u16*)d_in[2]; a.sk = (const u16*)d_in[3];
    a.ssm = (const u16*)d_in[4]; a.Dv = (const u16*)d_in[5];
    a.wg = (const u16*)d_in[7]; a.wu = (const u16*)d_in[8]; a.wd = (const u16*)d_in[9];
    a.ws = (char*)d_ws; a.out = (u16*)d_out;
    fused_kernel<<<NMAIN + NPREP + 1, 512, LDS_BYTES, stream>>>(a);
}
```
